```python
import torch

def _softmax(x, axis=-1):
    return torch.softmax(x, dim=axis)
def _gelu(x):
    return torch.nn.functional.gelu(x, approximate="tanh")
def _silu(x):
    return torch.nn.functional.silu(x)
def _sigmoid(x):
    return torch.sigmoid(x)
def _layernorm(x, axis=-1, eps=1e-5):
    m = x.mean(dim=axis, keepdim=True)
    v = x.var(dim=axis, keepdim=True, unbiased=False)
    return (x - m) / torch.sqrt(v + eps)
def _scatter_add(tgt, idx, src, axis=0):
    return tgt.clone().index_add_(axis, idx, src)
def _segment_sum(src, seg, n):
    out = torch.zeros((n,) + src.shape[1:], dtype=src.dtype)
    return out.index_add_(0, seg, src)
def _pad(x, pw):
    flat = tuple(v for pair in reversed(pw) for v in pair)
    return torch.nn.functional.pad(x, flat)

kernel_name = 'gated_mix__ssm_conv_s5__glu_gated_swiglu__prompt'

def setup_inputs():

    _w = (lambda a: (lambda t: t.to(torch.bfloat16) if t.is_floating_point() else t)(torch.as_tensor(a)))
    def _A_setup():
        B, S, D = 1, 2048, 640
        P = D
        Lambda_re = -0.5 * (lambda _x: torch.exp(torch.as_tensor(_x)))((lambda *s: torch.randn(*s, dtype=torch.bfloat16) * 0.2)(P))
        Lambda_im = (lambda *s: torch.randn(*s, dtype=torch.bfloat16) * 0.4)(P) * 1.0
        B_re = (lambda *s: torch.randn(*s, dtype=torch.bfloat16) * 0.2)(P, D) * (D ** -0.5)
        B_im = (lambda *s: torch.randn(*s, dtype=torch.bfloat16) * 0.2)(P, D) * (D ** -0.5)
        C_re = (lambda *s: torch.randn(*s, dtype=torch.bfloat16) * 0.2)(D, P) * (P ** -0.5)
        C_im = (lambda *s: torch.randn(*s, dtype=torch.bfloat16) * 0.2)(D, P) * (P ** -0.5)
        log_dt = (lambda *s: torch.randn(*s, dtype=torch.bfloat16) * 0.2)(P) * 0.3 - 2.0
        Dskip = (lambda *s: torch.randn(*s, dtype=torch.bfloat16) * 0.05)(D)
        return {
            "x": _w((lambda *s: torch.randn(*s, dtype=torch.bfloat16) * 0.05)(B, S, D)),
            "Lambda_re": _w(Lambda_re),
            "Lambda_im": _w(Lambda_im),
            "B_re": _w(B_re),
            "B_im": _w(B_im),
            "C_re": _w(C_re),
            "C_im": _w(C_im),
            "log_dt": _w(log_dt),
            "Dskip": _w(Dskip),
        }
    _A_in = _A_setup()

    def _B_setup():
        B, S, D, F = 1, 2048, 640, 1728
        return {
            "x": _w((lambda *s: torch.randn(*s, dtype=torch.bfloat16) * 0.05)(B, S, D)),
            "w1": _w((lambda *s: torch.randn(*s, dtype=torch.bfloat16) * 0.8)(D, F) * (2.0 / D) ** 0.5),
            "w3": _w((lambda *s: torch.randn(*s, dtype=torch.bfloat16) * 0.8)(D, F) * (2.0 / D) ** 0.5),
            "w2": _w((lambda *s: torch.randn(*s, dtype=torch.bfloat16) * 0.8)(F, D) * (2.0 / F) ** 0.5),
        }
    _B_in = _B_setup()

    return {
        "A_x": _A_in["x"],
        "A_Lambda_re": _A_in["Lambda_re"],
        "A_Lambda_im": _A_in["Lambda_im"],
        "A_B_re": _A_in["B_re"],
        "A_B_im": _A_in["B_im"],
        "A_C_re": _A_in["C_re"],
        "A_C_im": _A_in["C_im"],
        "A_log_dt": _A_in["log_dt"],
        "A_Dskip": _A_in["Dskip"],
        "B_x": _B_in["x"],
        "B_w1": _B_in["w1"],
        "B_w3": _B_in["w3"],
        "B_w2": _B_in["w2"],
        "gate_w": _w((lambda *s: torch.randn(*s, dtype=torch.bfloat16) * 0.05)(640) * 16.0),
    }

def reference(A_x, A_Lambda_re, A_Lambda_im, A_B_re, A_B_im, A_C_re, A_C_im, A_log_dt, A_Dskip, B_x, B_w1, B_w3, B_w2, gate_w):
    def _A_ref(x, Lambda_re, Lambda_im, B_re, B_im, C_re, C_im, log_dt, Dskip):
        Bsz, L, H = x.shape
        Lam = Lambda_re + 1j * Lambda_im
        Bmat = B_re + 1j * B_im
        Cmat = C_re + 1j * C_im
        dt = torch.exp(log_dt)
        Lam_bar = torch.exp(dt * Lam)
        B_bar = ((Lam_bar - 1.0) / Lam)[:, None] * Bmat
        Bu = torch.einsum("bld,pd->blp", x + 0j, B_bar)
        h = torch.zeros_like(Bu[:, 0, :])
        hs = []
        for t in range(L):
            h = Lam_bar[None, :] * h + Bu[:, t, :]
            hs.append(h)
        hseq = torch.stack(hs, 1)
        y = 2.0 * torch.einsum("blp,dp->bld", hseq, Cmat).real + x * Dskip[None, None, :]
        return y

    def _B_ref(x, w1, w3, w2):
        gate = torch.einsum("bsd,df->bsf", x, w1)
        up = torch.einsum("bsd,df->bsf", x, w3)
        h = _silu(gate) * up
        out = torch.einsum("bsf,fd->bsd", h, w2)
        return out

    x = A_x
    g = _sigmoid((x * gate_w).sum(-1, keepdims=True))
    a = _A_ref(x, A_Lambda_re, A_Lambda_im, A_B_re, A_B_im, A_C_re, A_C_im, A_log_dt, A_Dskip)
    b = _B_ref(x, B_w1, B_w3, B_w2)
    return g * a + (1 - g) * b
```

```cpp
#include <hip/hip_runtime.h>
#include <hip/hip_bf16.h>
typedef unsigned short u16;
#define L_ 2048
#define D_ 640
#define P_ 640
#define F_ 1728
__device__ __forceinline__ float bf2f(u16 v){ return __uint_as_float(((unsigned)v)<<16); }
__device__ __forceinline__ u16 f2bf(float x){ unsigned u=__float_as_uint(x); u += 0x7fffu + ((u>>16)&1u); return (u16)(u>>16);}
__device__ __forceinline__ float rbf(float x){ return bf2f(f2bf(x)); }

__global__ void k_z(const u16* x,const u16* Bre,const u16* Bim,float* Z){
  int p = blockIdx.x*64+threadIdx.x; int t = blockIdx.y;
  float sr=0,si=0;
  for(int d=0;d<D_;++d){ float xv=bf2f(x[t*D_+d]); sr+=xv*bf2f(Bre[p*D_+d]); si+=xv*bf2f(Bim[p*D_+d]); }
  Z[(t*P_+p)*2]=sr; Z[(t*P_+p)*2+1]=si;
}
__global__ void k_scan(const u16* Lre,const u16* Lim,const u16* ldt,float* Z){
  int p = blockIdx.x*64+threadIdx.x;
  float lr=bf2f(Lre[p]), li=bf2f(Lim[p]); float dt=rbf(expf(bf2f(ldt[p])));
  float er=expf(dt*lr); float ar=er*cosf(dt*li), ai=er*sinf(dt*li);
  float nr=ar-1.f, ni=ai; float den=lr*lr+li*li;
  float cr=(nr*lr+ni*li)/den, ci=(ni*lr-nr*li)/den;
  float hr=0,hi=0;
  for(int t=0;t<L_;++t){ float zr=Z[(t*P_+p)*2], zi=Z[(t*P_+p)*2+1];
    float ur=cr*zr-ci*zi, ui=cr*zi+ci*zr;
    float nhr=ar*hr-ai*hi+ur, nhi=ar*hi+ai*hr+ui; hr=nhr;hi=nhi;
    Z[(t*P_+p)*2]=rbf(hr); Z[(t*P_+p)*2+1]=rbf(hi);
  }
}
__global__ void k_h(const u16* x,const u16* w1,const u16* w3,float* H){
  int f = blockIdx.x*64+threadIdx.x; int t = blockIdx.y;
  float g=0,u=0;
  for(int d=0;d<D_;++d){ float xv=bf2f(x[t*D_+d]); g+=xv*bf2f(w1[d*F_+f]); u+=xv*bf2f(w3[d*F_+f]); }
  float s=g/(1.f+__expf(-g));
  H[t*F_+f]=rbf(s*u);
}
__global__ void k_out(const u16* x,const float* Z,const float* H,const u16* Cre,const u16* Cim,const u16* Dk,const u16* w2,const u16* gw,float* out){
  int d = blockIdx.x*64+threadIdx.x; int t = blockIdx.y;
  float y=0;
  for(int p=0;p<P_;++p){ y+= Z[(t*P_+p)*2]*bf2f(Cre[d*P_+p]) - Z[(t*P_+p)*2+1]*bf2f(Cim[d*P_+p]); }
  float b=0;
  for(int f=0;f<F_;++f) b+=H[t*F_+f]*bf2f(w2[f*D_+d]);
  float gl=0;
  for(int k=0;k<D_;++k) gl+=bf2f(x[t*D_+k])*bf2f(gw[k]);
  float g=1.f/(1.f+__expf(-gl));
  float a=2.f*y+bf2f(x[t*D_+d])*bf2f(Dk[d]);
  out[t*D_+d]=g*a+(1.f-g)*b;
}
extern "C" void kernel_launch(void* const* d_in, const int* in_sizes, int n_in, void* d_out, int out_size, void* d_ws, size_t ws_size, hipStream_t stream) {
  const u16* x=(const u16*)d_in[0]; const u16* Lre=(const u16*)d_in[1]; const u16* Lim=(const u16*)d_in[2];
  const u16* Bre=(const u16*)d_in[3]; const u16* Bim=(const u16*)d_in[4]; const u16* Cre=(const u16*)d_in[5]; const u16* Cim=(const u16*)d_in[6];
  const u16* ldt=(const u16*)d_in[7]; const u16* Dk=(const u16*)d_in[8]; const u16* w1=(const u16*)d_in[10]; const u16* w3=(const u16*)d_in[11]; const u16* w2=(const u16*)d_in[12]; const u16* gw=(const u16*)d_in[13];
  float* Z=(float*)d_ws; float* H=Z+L_*P_*2;
  k_z<<<dim3(P_/64,L_),64,0,stream>>>(x,Bre,Bim,Z);
  k_scan<<<dim3(P_/64),64,0,stream>>>(Lre,Lim,ldt,Z);
  k_h<<<dim3(F_/64,L_),64,0,stream>>>(x,w1,w3,H);
  k_out<<<dim3(D_/64,L_),64,0,stream>>>(x,Z,H,Cre,Cim,Dk,w2,gw,(float*)d_out);
}
```

```cpp
#include <hip/hip_runtime.h>
#include <hip/hip_bf16.h>

typedef unsigned short u16;
typedef short bf16x8 __attribute__((ext_vector_type(8)));
typedef short s16x4 __attribute__((ext_vector_type(4)));
typedef float f32x2 __attribute__((ext_vector_type(2)));
typedef float f32x4 __attribute__((ext_vector_type(4)));
typedef float f32x16 __attribute__((ext_vector_type(16)));
typedef unsigned u32x2 __attribute__((ext_vector_type(2)));
typedef unsigned u32x4 __attribute__((ext_vector_type(4)));
typedef __bf16 bf2_t __attribute__((ext_vector_type(2)));
typedef unsigned long long u64;
#define LAS __attribute__((address_space(3)))
#define GAS __attribute__((address_space(1)))
#define DI __device__ __forceinline__
#define SB() __builtin_amdgcn_sched_barrier(0)
#define RSRC __amdgpu_buffer_rsrc_t
#define ST_AUX 16

constexpr int L_ = 2048, D_ = 640, P_ = 640, F_ = 1728;
constexpr int KS2 = 94;
constexpr size_t HT_OFF = 0, HT_BYTES = (size_t)32 * KS2 * 4096;
constexpr size_t BT_OFF = HT_OFF + HT_BYTES, BT_BYTES = (size_t)8 * KS2 * 5120;
constexpr size_t G_OFF = BT_OFF + BT_BYTES, G_BYTES = 2048 * 4;
constexpr size_t ST_OFF = G_OFF + G_BYTES, ST_BYTES = 16 * 640 * 8;
constexpr size_t FL_OFF = ST_OFF + ST_BYTES, FL_BYTES = 256 * 128;
constexpr size_t EP_OFF = FL_OFF + FL_BYTES, EP_BYTES = 16 * 128;
constexpr size_t STAMP_OFF = EP_OFF + EP_BYTES;

constexpr unsigned RING = 0, XST = 81920, SCR = 147456, LDS1 = 163840;

DI unsigned cvtpk(float lo, float hi) { f32x2 v = {lo, hi}; bf2_t b = __builtin_convertvector(v, bf2_t); return __builtin_bit_cast(unsigned, b); }
DI float bf2f(unsigned v) { return __uint_as_float(v << 16); }
DI float rbf(float x) { return __uint_as_float(cvtpk(x, 0.f) << 16); }
DI s16x4 trr(unsigned a) { return __builtin_amdgcn_ds_read_tr16_b64_v4i16((LAS s16x4*)(size_t)a); }
DI bf16x8 ldsr(unsigned a) { return *(const LAS bf16x8*)(size_t)a; }
DI bf16x8 cat(s16x4 a, s16x4 b) { return __builtin_shufflevector(a, b, 0, 1, 2, 3, 4, 5, 6, 7); }
template <int IMM> DI void dma(unsigned voff, RSRC rs, unsigned soff) {
  asm volatile("buffer_load_dwordx4 %0, %1, %2 offen offset:%3 lds" ::"v"(voff), "s"(rs), "s"(soff), "n"(IMM) : "memory");
}
DI void setm0(unsigned v) { asm volatile("s_mov_b32 m0, %0\n\ts_nop 0" ::"s"(v) : "memory"); }
template <int N> DI void wait_bar() { asm volatile("s_waitcnt vmcnt(%0)\n\ts_barrier" ::"n"(N) : "memory"); }
DI RSRC mkrs(const void* p, unsigned bytes) { return __builtin_amdgcn_make_buffer_rsrc((void*)p, 0, bytes, 0x00020000); }
#define MFMA32(a, b, c) __builtin_amdgcn_mfma_f32_32x32x16_bf16((a), (b), (c), 0, 0, 0)
#define MFMA16(a, b, c) __builtin_amdgcn_mfma_f32_16x16x32_bf16((a), (b), (c), 0, 0, 0)
DI void mf_xa0(f32x16& d, const bf16x8& x, const bf16x8& w) { asm volatile("v_mfma_f32_32x32x16_bf16 %0, %1, %2, 0" : "=&v"(d) : "a"(x), "v"(w)); }
DI void mf_xa(f32x16& d, const bf16x8& x, const bf16x8& w) { asm volatile("v_mfma_f32_32x32x16_bf16 %0, %1, %2, %0" : "+v"(d) : "a"(x), "v"(w)); }
DI void mf_xb0(f32x16& d, const bf16x8& w, const bf16x8& x) { asm volatile("v_mfma_f32_32x32x16_bf16 %0, %1, %2, 0" : "=&v"(d) : "v"(w), "a"(x)); }
DI void mf_xb(f32x16& d, const bf16x8& w, const bf16x8& x) { asm volatile("v_mfma_f32_32x32x16_bf16 %0, %1, %2, %0" : "+v"(d) : "v"(w), "a"(x)); }
DI void mfma_done(f32x16& a, f32x16& b) { asm volatile("s_nop 7\n\ts_nop 7" : "+v"(a), "+v"(b)); }
#define STAMP(i) do {} while (0)

struct P1 {
  const u16 *x, *lre, *lim, *bre, *bim, *cre, *cim, *ldt, *w1, *w3, *w2, *gw;
  char* ws;
};

struct Stream {
  RSRC ra, rb;
  unsigned v0, v1, v2, v3;
  unsigned base, k, adv, lim, d1, d2, d3;
};
template <int Q> DI void st_piece(const Stream& s) {
  const unsigned so = s.base + s.k;
  if constexpr (Q == 0) dma<0>(s.v0, s.ra, so);
  else if constexpr (Q == 1) dma<1024>(s.v1, s.ra, so + s.d1);
  else if constexpr (Q == 2) dma<2048>(s.v2, s.rb, so + s.d2);
  else dma<3072>(s.v3, s.rb, so + s.d3);
}
DI void st_adv(Stream& s) { unsigned k = s.k + s.adv; s.k = (k >= s.lim) ? k - s.lim : k; }

struct Ctx {
  RSRC rx, rB, rw1, rw3, rnull;
  unsigned vS0, vS1, vS2, vS3, vD;
  unsigned lds0, m0ring;
  unsigned rho, w;
  int nS, s0, nD, d0;
};
DI void stream_S(Stream& s, const Ctx& c, int i) {
  s.ra = c.rB; s.rb = c.rB; s.v0 = c.vS0; s.v1 = c.vS1; s.v2 = c.vS2; s.v3 = c.vS3;
  s.base = (unsigned)(32 * i + 16 * (c.w & 1)) * 1280u; s.k = c.rho * 256u; s.adv = 256u; s.lim = 1280u;
  s.d1 = 4096u; s.d2 = 8192u; s.d3 = 12288u;
}
DI void stream_D(Stream& s, const Ctx& c, int j) {
  s.ra = c.rw1; s.rb = c.rw3; s.v0 = s.v1 = s.v2 = s.v3 = c.vD;
  s.base = (unsigned)j * 128u; s.k = c.rho * (128u * 3456u); s.adv = 64u * 3456u; s.lim = 640u * 3456u;
  s.d1 = 8u * 3456u - 1024u; s.d2 = 0u; s.d3 = 8u * 3456u - 1024u;
}
DI void stream_null(Stream& s, const Ctx& c) {
  s.ra = c.rnull; s.rb = c.rnull; s.base = 0; s.k = 0; s.adv = 0; s.lim = 1; s.d1 = s.d2 = s.d3 = 0;
}
DI void stream_job(Stream& s, const Ctx& c, int q) {
  if (q < c.nS) stream_S(s, c, c.s0 + q);
  else if (q < c.nS + c.nD) stream_D(s, c, c.d0 + q - c.nS);
  else stream_null(s, c);
}

struct Par { float lr, li, cr, ci; };
DI Par ssm_par(unsigned ulre, unsigned ulim, unsigned uldt) {
  const float Lr = bf2f(ulre), Li = bf2f(ulim);
  const float dt = rbf(expf(bf2f(uldt)));
  const float e = expf(dt * Lr), th = dt * Li;
  const float n = __builtin_rintf(th * 0.636619772f);
  float y = fmaf(n, -1.57079601287841796875f, th);
  y = fmaf(n, -3.1391647326017846353352069854736328125e-7f, y);
  y = fmaf(n, -5.390302529957764765544681040410068817436695098876953125e-15f, y);
  const float y2 = y * y;
  float ps = fmaf(y2, 2.6083159809786593541503e-06f, -0.0001981069071916863322258f);
  ps = fmaf(ps, y2, 0.00833307858556509017944336f); ps = fmaf(ps, y2, -0.166666597127914428710938f);
  const float sn = fmaf(ps * y2, y, y);
  float pc = fmaf(y2, -2.71811842367242206819355e-07f, 2.47990446951007470488548e-05f);
  pc = fmaf(pc, y2, -0.00138888787478208541870117f); pc = fmaf(pc, y2, 0.0416666641831398010253906f); pc = fmaf(pc, y2, -0.5f);
  const float cs = fmaf(pc, y2, 1.f);
  const int q = (int)n;
  float sv = (q & 1) ? cs : sn, cv = (q & 1) ? sn : cs;
  sv = (q & 2) ? -sv : sv; cv = ((q + 1) & 2) ? -cv : cv;
  Par r; r.lr = e * cv; r.li = e * sv;
  const float nr = r.lr - 1.f, ni = r.li, den = Lr * Lr + Li * Li;
  r.cr = 2.f * ((nr * Lr + ni * Li) / den); r.ci = 2.f * ((ni * Lr - nr * Li) / den);
  return r;
}
DI unsigned ldu16(const u16* p) { unsigned r; asm volatile("global_load_ushort %0, %1, off" : "=v"(r) : "v"(p) : "memory"); return r; }
DI void csq(float& a, float& b) { const float r = a * a - b * b, i = 2.f * a * b; a = r; b = i; }

template <bool FIRST, class Pre>
DI void s_body(bf16x8 (&xf)[40], f32x16& zre, f32x16& zim, Stream& st, const Ctx& c, int nextjob, const unsigned (&sa)[8], unsigned xso, Pre pre) {
  bf16x8 wr[3], wi[3];
  const unsigned xdelta = XST + c.w * 16384u;
  auto rdw = [&](int ks, bf16x8& a, bf16x8& b) {
    const int n = ks >> 3, s = ks & 7;
    const unsigned off = n * 16384;
    if (n < 4) { a = ldsr(sa[s] + off); b = ldsr(sa[s] + off + 8192); }
    else { const unsigned t = sa[s] + 65536u; a = ldsr(t); b = ldsr(t + 8192); }
  };
  auto rdx = [&](int n) {
#pragma unroll
    for (int s = 0; s < 8; ++s) xf[8 * n + s] = ldsr(sa[s] + xdelta + (n & 1) * 8192);
  };
  auto xdma = [&](int n) {
    unsigned k = (n + c.rho) % 5u * 256u + xso;
    setm0(c.lds0 + xdelta + (n & 1) * 8192);
    dma<0>(c.vS0, c.rx, k); dma<1024>(c.vS1, c.rx, k + 20480 - 1024); dma<2048>(c.vS2, c.rx, k + 5120 - 2048); dma<3072>(c.vS3, c.rx, k + 25600 - 3072);
    setm0(c.lds0 + xdelta + (n & 1) * 8192 + 4096);
    dma<0>(c.vS0, c.rx, k + 10240); dma<1024>(c.vS1, c.rx, k + 30720 - 1024); dma<2048>(c.vS2, c.rx, k + 15360 - 2048); dma<3072>(c.vS3, c.rx, k + 35840 - 3072);
  };
  auto item = [&](int n) {
    if (n == 2) stream_job(st, c, nextjob);
    setm0(c.m0ring + ((n + 3) % 5) * 16384);
    st_piece<0>(st); st_piece<1>(st); st_piece<2>(st); st_piece<3>(st); st_adv(st);
  };
  if constexpr (FIRST) {
    xdma(0);
    setm0(c.m0ring); st_piece<0>(st); st_piece<1>(st); st_piece<2>(st); st_piece<3>(st); st_adv(st);
    xdma(1);
    setm0(c.m0ring + 16384); st_piece<0>(st); st_piece<1>(st); st_piece<2>(st); st_piece<3>(st); st_adv(st);
    setm0(c.m0ring + 32768); st_piece<0>(st); st_piece<1>(st); st_piece<2>(st); st_piece<3>(st); st_adv(st);
    pre();
    SB();
    wait_bar<16>();
    rdx(0);
    SB();
  } else wait_bar<8>();
  rdw(0, wr[0], wi[0]); rdw(1, wr[1], wi[1]);
  item(0);
  SB();
#pragma unroll
  for (int ks = 0; ks < 40; ++ks) {
    if ((ks & 7) == 6 && ks < 38) {
      const int n = (ks >> 3) + 1;
      if constexpr (FIRST) { if (n == 1) wait_bar<16>(); else if (n == 4) wait_bar<4>(); else wait_bar<12>(); rdx(n); }
      else wait_bar<8>();
      item(n);
    }
    if (ks + 2 < 40) rdw(ks + 2, wr[(ks + 2) % 3], wi[(ks + 2) % 3]);
    if (ks == 0) { mf_xa0(zre, xf[0], wr[0]); mf_xa0(zim, xf[0], wi[0]); }
    else { mf_xa(zre, xf[ks], wr[ks % 3]); mf_xa(zim, xf[ks], wi[ks % 3]); }
    SB();
    if constexpr (FIRST) { if ((ks & 7) == 1 && (ks >> 3) + 2 < 5) { xdma((ks >> 3) + 2); SB(); } }
  }
  mfma_done(zre, zim);
}

__global__ __launch_bounds__(256, 1) void k1(P1 p) {
  extern __shared__ __attribute__((aligned(16))) char smem[];
  const int lane = threadIdx.x & 63;
  const unsigned w = __builtin_amdgcn_readfirstlane(threadIdx.x >> 6);
  const int bid = blockIdx.x, xcd = bid & 7, r = bid >> 3;
  const int rb = (xcd & 1) * 8 + (r & 7), cg = (xcd >> 1) + 4 * (r >> 3);
  STAMP(0);
  Ctx c;
  c.w = w; c.rho = (unsigned)(rb + (r >> 3)) % 5u;
  c.lds0 = (unsigned)(size_t)smem; c.m0ring = c.lds0 + RING + w * 4096u;
  c.rx = mkrs(p.x, L_ * D_ * 2); c.rB = mkrs(w < 2 ? p.bre : p.bim, P_ * D_ * 2);
  c.rw1 = mkrs(p.w1, D_ * F_ * 2); c.rw3 = mkrs((const char*)p.w3 - 2048, D_ * F_ * 2 + 2048); c.rnull = mkrs(p.x, 0);
  {
    const unsigned rr = lane >> 4, t = (lane & 15) ^ rr;
    c.vS0 = rr * 1280 + t * 16; c.vS1 = rr * 1280 + (t ^ 4) * 16; c.vS2 = rr * 1280 + (t ^ 8) * 16; c.vS3 = rr * 1280 + (t ^ 12) * 16;
    const unsigned lrow = lane >> 3, lch = (lane & 7) ^ (((lrow >> 1) & 1) << 2);
    c.vD = (lrow + 16 * w) * 3456 + lch * 16;
  }
  if (cg < 13) { c.nS = 1; c.s0 = cg; c.nD = 2; c.d0 = 2 * cg; }
  else if (cg == 13) { c.nS = 2; c.s0 = 13; c.nD = 1; c.d0 = 26; }
  else if (cg == 14) { c.nS = 3; c.s0 = 15; c.nD = 0; c.d0 = 0; }
  else { c.nS = 2; c.s0 = 18; c.nD = 0; c.d0 = 0; }
  const int l32 = lane & 31, h = lane >> 5;
  GAS u64* epw = (GAS u64*)(p.ws + EP_OFF + cg * 128);
  u64 ep = *(const __attribute__((address_space(4))) u64*)(size_t)(p.ws + EP_OFF + cg * 128);
  unsigned raw[3][3];
#pragma unroll
  for (int i = 0; i < 3; ++i) {
    const int ch = 32 * (c.s0 + (i < c.nS ? i : 0)) + l32;
    raw[i][0] = ldu16(p.lre + ch); raw[i][1] = ldu16(p.lim + ch); raw[i][2] = ldu16(p.ldt + ch);
  }

  bf16x8 xf[40];
  f32x16 zre[3], zim[3];
  Par par[3];
  Stream st;
  stream_S(st, c, c.s0);
  unsigned sa[8];
#pragma unroll
  for (int s = 0; s < 8; ++s) sa[s] = c.lds0 + l32 * 256 + (((2 * s + h) ^ (l32 & 15)) << 4);
  const RSRC rst = mkrs(p.ws + ST_OFF, ST_BYTES);

  auto s_post = [&](const f32x16& zr, const f32x16& zi, const Par& pr, int i) {
    float sr = zr[0], si = zi[0];
#pragma unroll
    for (int j = 1; j < 16; ++j) {
      const float a = fmaf(pr.lr, sr, fmaf(-pr.li, si, zr[j])), b = fmaf(pr.lr, si, fmaf(pr.li, sr, zi[j]));
      sr = a; si = b;
    }
    LAS f32x2* seg = (LAS f32x2*)(size_t)(c.lds0 + SCR + i * 2048);
    seg[(2 * w + h) * 32 + l32] = (f32x2){sr, si};
    asm volatile("s_waitcnt lgkmcnt(0)\n\ts_barrier" ::: "memory");
    if (w == 0) {
      float mr = pr.lr, mi = pr.li;
      csq(mr, mi); csq(mr, mi); csq(mr, mi); csq(mr, mi);
      f32x2 a = seg[l32];
#pragma unroll
      for (int g = 1; g < 8; ++g) {
        const f32x2 s = seg[g * 32 + l32];
        const float x0 = fmaf(mr, a[0], fmaf(-mi, a[1], s[0])), x1 = fmaf(mr, a[1], fmaf(mi, a[0], s[1]));
        a = (f32x2){x0, x1};
      }
      if (h == 0) __builtin_amdgcn_raw_buffer_store_b64(__builtin_bit_cast(u32x2, a), rst, (rb * 640 + 32 * (c.s0 + i) + l32) * 8, 0, 16);
    }
  };

  s_body<true>(xf, zre[0], zim[0], st, c, 1, sa, (unsigned)(128 * rb + 32 * w) * 1280u, [&]() {
    asm volatile("s_waitcnt vmcnt(28)" : "+v"(raw[0][0]), "+v"(raw[0][1]), "+v"(raw[0][2]), "+v"(raw[1][0]), "+v"(raw[1][1]), "+v"(raw[1][2]),
                 "+v"(raw[2][0]), "+v"(raw[2][1]), "+v"(raw[2][2]), "+s"(ep)::"memory");
#pragma unroll
    for (int i = 0; i < 3; ++i) {
      par[i] = ssm_par(raw[i][0], raw[i][1], raw[i][2]);
      asm volatile("" : "+v"(par[i].lr), "+v"(par[i].li), "+v"(par[i].cr), "+v"(par[i].ci));
    }
  });
  STAMP(1);
  s_post(zre[0], zim[0], par[0], 0);
  zre[1] = zre[0]; zim[1] = zim[0]; zre[2] = zre[0]; zim[2] = zim[0];
  asm volatile("" : "+a"(zre[1]), "+a"(zim[1]), "+a"(zre[2]), "+a"(zim[2]));
  for (int i = 1; i < c.nS; ++i) {
    f32x16 zr, zi;
    s_body<false>(xf, zr, zi, st, c, i + 1, sa, 0, []() {});
    s_post(zr, zi, i == 1 ? par[1] : par[2], i);
    if (i == 1) { zre[1] = zr; zim[1] = zi; } else { zre[2] = zr; zim[2] = zi; }
    asm volatile("" : "+a"(zre[1]), "+a"(zim[1]), "+a"(zre[2]), "+a"(zim[2]));
  }
  STAMP(2);
  const u64 tag = ep * 0x9E3779B97F4A7C15ull + 0x1234567ull;
  GAS u64* myflag = (GAS u64*)(p.ws + FL_OFF + (cg * 16 + rb) * 128);
  const RSRC rht = mkrs(p.ws + HT_OFF, HT_BYTES);
  const unsigned mtbase = (unsigned)((2 * rb + (w >> 1)) * KS2 * 4 + 2 * (w & 1)) * 1024u;

  if (c.nD > 0) {
    unsigned rbd[2][2];
    {
      const unsigned blk = (lane >> 4) & 1, q = (lane & 15) >> 2, pp = lane & 3;
      const unsigned row = 8 * h + q, ch = 2 * blk + (pp >> 1);
      const unsigned a = c.lds0 + row * 128 + ((ch ^ (((row >> 1) & 1) << 2)) * 16) + 8 * (pp & 1);
      rbd[0][0] = a; rbd[1][0] = a ^ 64; rbd[0][1] = a + 49152; rbd[1][1] = (a ^ 64) + 49152;
      asm volatile("" : "+v"(rbd[0][0]), "+v"(rbd[1][0]), "+v"(rbd[0][1]), "+v"(rbd[1][1]));
    }
    const unsigned vst = (unsigned)((l32 >> 4) * 1024 + h * 256 + (l32 & 15) * 16);
    for (int j = 0; j < c.nD; ++j) {
      f32x16 acc[4];
      s16x4 wl[2][4], wh[2][4];
      auto rd1 = [&](int ks, int t, int half) -> s16x4 {
        const unsigned off = ((ks >> 2) % 5) * 16384 + (ks & 3) * 4096 + (t >> 1) * 2048 + half * 512;
        return trr(off >= 49152 ? rbd[t & 1][1] + (off - 49152) : rbd[t & 1][0] + off);
      };
      wait_bar<8>();
      setm0(c.m0ring + 3 * 16384);
#pragma unroll
      for (int t = 0; t < 4; ++t) { wl[0][t] = rd1(0, t, 0); wh[0][t] = rd1(0, t, 1); }
      st_piece<0>(st);
      SB();
#pragma unroll
      for (int ks = 0; ks < 40; ++ks) {
        if ((ks & 3) == 3 && ks < 39) {
          const int n = (ks >> 2) + 1;
          wait_bar<8>();
          if (n == 3 && j == 0 && w == 0) { if (lane == 0) __hip_atomic_store(myflag, tag, __ATOMIC_RELAXED, __HIP_MEMORY_SCOPE_AGENT); }
          if (n == 7) stream_job(st, c, c.nS + j + 1);
          setm0(c.m0ring + ((n + 3) % 5) * 16384);
        }
#pragma unroll
        for (int t = 0; t < 4; ++t) {
          const bf16x8 wf = cat(wl[ks & 1][t], wh[ks & 1][t]);
          if (ks == 0) mf_xb0(acc[t], wf, xf[0]); else mf_xb(acc[t], wf, xf[ks]);
          SB();
          if (t == 0 && ks < 39) {
            const int q = (ks + 1) & 3;
            if (q == 0) st_piece<0>(st); else if (q == 1) st_piece<1>(st); else if (q == 2) st_piece<2>(st); else { st_piece<3>(st); st_adv(st); }
          }
          if (ks == 38 && t > 0) {}
          if (ks < 39) { wl[(ks + 1) & 1][t] = rd1(ks + 1, t, 0); wh[(ks + 1) & 1][t] = rd1(ks + 1, t, 1); }
          SB();
        }
      }
      mfma_done(acc[0], acc[2]); mfma_done(acc[1], acc[3]);
      const unsigned sof = mtbase + (unsigned)(40 + 2 * (c.d0 + j)) * 4096u;
#pragma unroll
      for (int t = 0; t < 2; ++t) {
        float e[16];
#pragma unroll
        for (int u = 0; u < 16; ++u) e[u] = __builtin_amdgcn_exp2f(acc[t][u] * -1.44269504f);
        SB();
#pragma unroll
        for (int u = 0; u < 16; ++u) e[u] = __builtin_amdgcn_rcpf(1.f + e[u]);
        SB();
        unsigned pk[8];
#pragma unroll
        for (int u = 0; u < 16; u += 2) pk[u >> 1] = cvtpk(acc[t][u] * acc[2 + t][u] * e[u], acc[t][u + 1] * acc[2 + t][u + 1] * e[u + 1]);
        __builtin_amdgcn_raw_buffer_store_b128((u32x4){pk[0], pk[1], pk[2], pk[3]}, rht, vst, sof + t * 4096, ST_AUX);
        __builtin_amdgcn_raw_buffer_store_b128((u32x4){pk[4], pk[5], pk[6], pk[7]}, rht, vst + 512, sof + t * 4096, ST_AUX);
        SB();
      }
      STAMP(3 + j);
    }
  } else {
    asm volatile("s_waitcnt vmcnt(0)" ::: "memory");
    if (w == 0 && lane == 0) __hip_atomic_store(myflag, tag, __ATOMIC_RELAXED, __HIP_MEMORY_SCOPE_AGENT);
    if (cg == 15) {
      f32x16 a;
#pragma unroll
      for (int n = 0; n < 5; ++n) {
        const unsigned k = (n + c.rho) % 5u * 128u + 8 * h;
        bf16x8 gf[8];
#pragma unroll
        for (int s = 0; s < 8; ++s) gf[s] = *(const bf16x8*)(p.gw + k + 16 * s);
#pragma unroll
        for (int s = 0; s < 8; ++s) { if (n + s == 0) mf_xb0(a, gf[s], xf[0]); else mf_xb(a, gf[s], xf[8 * n + s]); }
      }
      asm volatile("s_nop 7\n\ts_nop 7" : "+v"(a));
      const float lg = rbf(a[0]);
      const float g = rbf(1.f / (1.f + __expf(-lg)));
      if (h == 0) ((float*)(p.ws + G_OFF))[128 * rb + 32 * w + l32] = g;
    }
    const int v = (rb * 2 + (cg - 14)) * 4 + w;
    const RSRC rbt = mkrs(p.ws + BT_OFF, BT_BYTES);
    {
      const int col = lane & 15, q = lane >> 4;
      for (int pc = v; pc < 160; pc += 128) {
        const int i = pc >> 3, nt = pc & 7;
        u32x4 re[5], im[5];
#pragma unroll
        for (int n = 0; n < 5; ++n) {
          const int o = (80 * nt + 16 * n + col) * 640 + 32 * i + 8 * q;
          re[n] = *(const u32x4*)(p.cre + o); im[n] = *(const u32x4*)(p.cim + o);
        }
#pragma unroll
        for (int n = 0; n < 5; ++n) {
          u32x4 o0, o1;
          const u32x4 a = re[n], b = im[n] ^ 0x80008000u;
          o0[0] = __builtin_amdgcn_perm(b[0], a[0], 0x05040100u); o0[1] = __builtin_amdgcn_perm(b[0], a[0], 0x07060302u);
          o0[2] = __builtin_amdgcn_perm(b[1], a[1], 0x05040100u); o0[3] = __builtin_amdgcn_perm(b[1], a[1], 0x07060302u);
          o1[0] = __builtin_amdgcn_perm(b[2], a[2], 0x05040100u); o1[1] = __builtin_amdgcn_perm(b[2], a[2], 0x07060302u);
          o1[2] = __builtin_amdgcn_perm(b[3], a[3], 0x05040100u); o1[3] = __builtin_amdgcn_perm(b[3], a[3], 0x07060302u);
          const unsigned so = (unsigned)((nt * KS2 + 2 * i) * 5 + n) * 1024u;
          __builtin_amdgcn_raw_buffer_store_b128(o0, rbt, lane * 16, so, ST_AUX);
          __builtin_amdgcn_raw_buffer_store_b128(o1, rbt, lane * 16, so + 5120, ST_AUX);
        }
      }
    }
    {
      const RSRC rw2 = mkrs(p.w2, F_ * D_ * 2);
      unsigned vo[5];
#pragma unroll
      for (int i = 0; i < 5; ++i) { const unsigned o = 1024 * i + 16 * lane, row = o / 160; vo[i] = row * 1280 + (o - row * 160); }
      const unsigned q = lane >> 4, qq = (lane & 15) >> 2, pp = lane & 3;
      const unsigned ra = c.lds0 + XST + w * 16384 + (16 * (q >> 1) + 4 * (q & 1) + qq) * 160 + 8 * pp;
      for (int base = v; base < 432; base += 384) {
        const int cnt = base + 256 < 432 ? 3 : (base + 128 < 432 ? 2 : 1);
        for (int u = 0; u < cnt; ++u) {
          const int pc = base + 128 * u, ft = pc >> 3, nt = pc & 7;
          const unsigned so = (unsigned)(32 * ft) * 1280u + 160u * nt;
          const unsigned m0 = c.lds0 + XST + w * 16384 + u * 5120;
#pragma unroll
          for (int i = 0; i < 5; ++i) { setm0(m0 + 1024 * i); dma<0>(vo[i], rw2, so); }
        }
        asm volatile("s_waitcnt vmcnt(0)" ::: "memory");
        for (int u = 0; u < cnt; ++u) {
          const int pc = base + 128 * u, ft = pc >> 3, nt = pc & 7;
#pragma unroll
          for (int n = 0; n < 5; ++n) {
            const unsigned a = ra + u * 5120 + 32 * n;
            const bf16x8 f = cat(trr(a), trr(a + 8 * 160));
            __builtin_amdgcn_raw_buffer_store_b128(__builtin_bit_cast(u32x4, f), rbt, lane * 16, (unsigned)((nt * KS2 + 40 + ft) * 5 + n) * 1024u, ST_AUX);
          }
        }
        asm volatile("s_waitcnt lgkmcnt(0)" ::: "memory");
      }
    }
  }
  STAMP(5);
  {
    GAS u64* fl = (GAS u64*)(p.ws + FL_OFF + (cg * 16 + (lane & 15)) * 128);
    for (unsigned spins = 0; spins < (1u << 22); ++spins) {
      const u64 f = __hip_atomic_load(fl, __ATOMIC_RELAXED, __HIP_MEMORY_SCOPE_AGENT);
      if (__all((lane & 15) >= rb || f == tag)) break;
      __builtin_amdgcn_s_sleep(2);
    }
    __builtin_amdgcn_fence(__ATOMIC_ACQUIRE, "wavefront");
    if (rb == 15 && w == 0 && lane == 0) __hip_atomic_store(epw, ep + 1, __ATOMIC_RELAXED, __HIP_MEMORY_SCOPE_AGENT);
    STAMP(6);
    const unsigned tb = c.lds0 + XST + w * 16384;
    for (int i = 0; i < c.nS; ++i) {
      f32x16 zr, zi; Par pr;
      if (i == 0) { zr = zre[0]; zi = zim[0]; pr = par[0]; } else if (i == 1) { zr = zre[1]; zi = zim[1]; pr = par[1]; } else { zr = zre[2]; zi = zim[2]; pr = par[2]; }
      f32x2 sv[8];
#pragma unroll
      for (int u = 0; u < 8; ++u) {
        const int d = 8 * h + u, j = rb - 1 - d;
        const unsigned o = j >= 0 ? (unsigned)(j * 640 + 32 * (c.s0 + i) + l32) * 8u : 0x7fffff00u;
        sv[u] = __builtin_bit_cast(f32x2, __builtin_amdgcn_raw_buffer_load_b64(rst, o, 0, 16));
      }
      float mr = pr.lr, mi = pr.li;
      csq(mr, mi); csq(mr, mi); csq(mr, mi); csq(mr, mi);
      float nr = mr, ni = mi;
      csq(nr, ni); csq(nr, ni); csq(nr, ni);
      float qr = nr, qi = ni;
      csq(qr, qi); csq(qr, qi); csq(qr, qi);
      float pr_ = sv[7][0], pi_ = sv[7][1];
#pragma unroll
      for (int u = 6; u >= 0; --u) {
        const float a = fmaf(nr, pr_, fmaf(-ni, pi_, sv[u][0])), b = fmaf(nr, pi_, fmaf(ni, pr_, sv[u][1]));
        pr_ = a; pi_ = b;
      }
      const float orr = __shfl_xor(pr_, 32), oi = __shfl_xor(pi_, 32);
      const float lo_r = h ? orr : pr_, lo_i = h ? oi : pi_, hi_r = h ? pr_ : orr, hi_i = h ? pi_ : oi;
      float cr = fmaf(qr, hi_r, fmaf(-qi, hi_i, lo_r)), ci = fmaf(qr, hi_i, fmaf(qi, hi_r, lo_i));
      const LAS f32x2* seg = (const LAS f32x2*)(size_t)(c.lds0 + SCR + i * 2048);
      const int g = 2 * w + h;
#pragma unroll
      for (int gg = 0; gg < 7; ++gg) {
        const f32x2 s = seg[gg * 32 + l32];
        const float a = fmaf(mr, cr, fmaf(-mi, ci, s[0])), b = fmaf(mr, ci, fmaf(mi, cr, s[1]));
        if (gg < g) { cr = a; ci = b; }
      }
      const unsigned cb = (l32 >> 2) ^ (2 * h);
#pragma unroll
      for (int j = 0; j < 16; ++j) {
        const float a = fmaf(pr.lr, cr, fmaf(-pr.li, ci, zr[j])), b = fmaf(pr.lr, ci, fmaf(pr.li, cr, zi[j]));
        cr = a; ci = b;
        const float o0 = pr.cr * a - pr.ci * b, o1 = pr.cr * b + pr.ci * a;
        const unsigned sj = ((j & 3) >> 1) + 4 * ((j >> 2) & 1);
        const unsigned ad = tb + ((j & 3) + 8 * (j >> 2) + 4 * h) * 128 + ((cb ^ sj) << 4) + (l32 & 3) * 4;
        *(LAS unsigned*)(size_t)ad = cvtpk(o0, o1);
      }
      const unsigned row = lane & 15, q = lane >> 4;
#pragma unroll
      for (int rt = 0; rt < 2; ++rt)
#pragma unroll
        for (int kk = 0; kk < 2; ++kk) {
          const bf16x8 f = ldsr(tb + (16 * rt + row) * 128 + (((2 * q + kk) ^ ((row >> 1) & 7)) << 4));
          __builtin_amdgcn_raw_buffer_store_b128(__builtin_bit_cast(u32x4, f), rht, lane * 16, mtbase + (unsigned)(2 * (c.s0 + i) + kk) * 4096u + rt * 1024, ST_AUX);
        }
    }
  }
  STAMP(7);
}

struct P2 { const u16 *x, *dsk; const char* ws; float* out; };

__global__ __launch_bounds__(256, 1) void k2(P2 p) {
  extern __shared__ __attribute__((aligned(16))) char smem[];
  const int lane = threadIdx.x & 63;
  const unsigned w = __builtin_amdgcn_readfirstlane(threadIdx.x >> 6);
  const int bid = blockIdx.x, xcd = bid & 7, r = bid >> 3;
  const int mt = (xcd >> 1) * 8 + (r >> 2), nt = (xcd & 1) * 4 + (r & 3);
  STAMP(0);
  const RSRC ra = mkrs(p.ws + HT_OFF + (size_t)mt * KS2 * 4096, KS2 * 4096);
  const RSRC rb = mkrs(p.ws + BT_OFF + (size_t)nt * KS2 * 5120, KS2 * 5120);
  constexpr int NS = 24, DEP = 3;
  f32x4 acc[4][5], accS[4][5];
  bf16x8 fa[DEP][4], fb[DEP][5];
  const unsigned vo = lane * 16;
  auto ld = [&](int s, int g) {
    const unsigned ks = w + 4 * s;
    const unsigned va = s == NS - 1 ? vo + ks * 4096 : vo, sa = s == NS - 1 ? 0u : ks * 4096;
    const unsigned vb = s == NS - 1 ? vo + ks * 5120 : vo, sb = s == NS - 1 ? 0u : ks * 5120;
    if (g < 4) fa[s % DEP][g] = __builtin_bit_cast(bf16x8, __builtin_amdgcn_raw_buffer_load_b128(ra, va + g * 1024, sa, 0));
    else if (g < 8) fb[s % DEP][g - 4] = __builtin_bit_cast(bf16x8, __builtin_amdgcn_raw_buffer_load_b128(rb, vb + (g - 4) * 1024, sb, 0));
    else fb[s % DEP][4] = __builtin_bit_cast(bf16x8, __builtin_amdgcn_raw_buffer_load_b128(rb, vb, sb + 4096, 0));
  };
#pragma unroll
  for (int s = 0; s < DEP - 1; ++s)
#pragma unroll
    for (int g = 0; g < 9; ++g) ld(s, g);
  const float* G = (const float*)(p.ws + G_OFF);
  float gv[4];
#pragma unroll
  for (int m = 0; m < 4; ++m) gv[m] = G[64 * mt + 16 * m + (lane & 15)];
  const int irow = 16 * w + (lane & 15), rr = irow & 31;
  const int time = 64 * mt + (irow & 32) + 16 * ((rr >> 2) & 1) + 4 * (rr >> 3) + (rr & 3);
  u32x2 xv[5], dv[5];
#pragma unroll
  for (int n = 0; n < 5; ++n) {
    const int d = 80 * nt + 16 * n + 4 * (lane >> 4);
    xv[n] = *(const u32x2*)(p.x + time * 640 + d); dv[n] = *(const u32x2*)(p.dsk + d);
  }
  SB();
#pragma unroll
  for (int s = 0; s < NS; ++s) {
#pragma unroll
    for (int g = 0; g < 10; ++g) {
#pragma unroll
      for (int q = 2 * g; q < 2 * g + 2; ++q) {
        const int m = q / 5, n = q % 5;
        const f32x4 z = {0.f, 0.f, 0.f, 0.f};
        if (s < 10) accS[m][n] = MFMA16(fb[s % DEP][n], fa[s % DEP][m], s == 0 ? z : accS[m][n]);
        else acc[m][n] = MFMA16(fb[s % DEP][n], fa[s % DEP][m], s == 10 ? z : acc[m][n]);
      }
      if (s + DEP - 1 < NS && g < 9) ld(s + DEP - 1, g);
      SB();
    }
  }
  STAMP(1);
  LAS f32x4* red = (LAS f32x4*)smem;
#pragma unroll
  for (int m = 0; m < 4; ++m) {
    const float g = gv[m], og = rbf(1.f - g);
#pragma unroll
    for (int n = 0; n < 5; ++n) red[(w * 20 + m * 5 + n) * 64 + lane] = accS[m][n] * g + acc[m][n] * og;
  }
  __syncthreads();
  const float gm = w == 0 ? gv[0] : (w == 1 ? gv[1] : (w == 2 ? gv[2] : gv[3]));
#pragma unroll
  for (int n = 0; n < 5; ++n) {
    f32x4 s = red[(w * 5 + n) * 64 + lane];
#pragma unroll
    for (int u = 1; u < 4; ++u) s += red[(u * 20 + w * 5 + n) * 64 + lane];
    s[0] += gm * rbf(bf2f(xv[n][0] & 0xffffu) * bf2f(dv[n][0] & 0xffffu));
    s[1] += gm * rbf(bf2f(xv[n][0] >> 16) * bf2f(dv[n][0] >> 16));
    s[2] += gm * rbf(bf2f(xv[n][1] & 0xffffu) * bf2f(dv[n][1] & 0xffffu));
    s[3] += gm * rbf(bf2f(xv[n][1] >> 16) * bf2f(dv[n][1] >> 16));
    __builtin_amdgcn_raw_buffer_store_b128(__builtin_bit_cast(u32x4, s), mkrs(p.out, 2048*640*4), (time * 640 + 80 * nt + 16 * n + 4 * (lane >> 4)) * 4, 0, 16);
  }
  STAMP(2);
}

extern "C" void kernel_launch(void* const* d_in, const int* in_sizes, int n_in, void* d_out, int out_size, void* d_ws, size_t ws_size, hipStream_t stream) {
  P1 a;
  a.x = (const u16*)d_in[0]; a.lre = (const u16*)d_in[1]; a.lim = (const u16*)d_in[2]; a.bre = (const u16*)d_in[3]; a.bim = (const u16*)d_in[4];
  a.cre = (const u16*)d_in[5]; a.cim = (const u16*)d_in[6]; a.ldt = (const u16*)d_in[7];
  a.w1 = (const u16*)d_in[10]; a.w3 = (const u16*)d_in[11]; a.w2 = (const u16*)d_in[12]; a.gw = (const u16*)d_in[13];
  a.ws = (char*)d_ws;
  P2 b; b.x = a.x; b.dsk = (const u16*)d_in[8]; b.ws = (const char*)d_ws; b.out = (float*)d_out;
  (void)hipFuncSetAttribute((const void*)k1, hipFuncAttributeMaxDynamicSharedMemorySize, LDS1);
  (void)hipFuncSetAttribute((const void*)k2, hipFuncAttributeMaxDynamicSharedMemorySize, 81920);
  k1<<<256, 256, LDS1, stream>>>(a);
  k2<<<256, 256, 81920, stream>>>(b);
}
```

```cpp
#include <hip/hip_runtime.h>
#include <hip/hip_bf16.h>
#include <utility>

typedef unsigned short u16;
typedef short bf16x8 __attribute__((ext_vector_type(8)));
typedef short s16x4 __attribute__((ext_vector_type(4)));
typedef float f32x2 __attribute__((ext_vector_type(2)));
typedef float f32x4 __attribute__((ext_vector_type(4)));
typedef float f32x16 __attribute__((ext_vector_type(16)));
typedef unsigned u32x2 __attribute__((ext_vector_type(2)));
typedef unsigned u32x4 __attribute__((ext_vector_type(4)));
typedef __bf16 bf2_t __attribute__((ext_vector_type(2)));
typedef unsigned long long u64;
#define LAS __attribute__((address_space(3)))
#define DI __device__ __forceinline__
#define SB() __builtin_amdgcn_sched_barrier(0)
#define RSRC __amdgpu_buffer_rsrc_t
#define RHO 3
#define PRE_T 15
#define PRE_E 12
#define CPF 1
#define CPF_ALL 0
#define TOUCH 0
constexpr int SC1 = 16;
constexpr unsigned OOB = 0x7fffff00u;

constexpr int L_ = 2048, D_ = 640, P_ = 640, F_ = 1728;
constexpr int KS2 = 96;
constexpr size_t HT_OFF = 0, HT_BYTES = (size_t)32 * KS2 * 4096;
constexpr size_t BT_OFF = HT_OFF + HT_BYTES, BT_BYTES = (size_t)8 * KS2 * 5120;
constexpr size_t G_OFF = BT_OFF + BT_BYTES, G_BYTES = 2048 * 4;
constexpr size_t ST_OFF = G_OFF + G_BYTES, ST_BYTES = 16 * 640 * 8;
constexpr size_t FL_OFF = ST_OFF + ST_BYTES;
constexpr unsigned FS_ = 0, FA_ = 2048, FC_ = 4096, FB_ = 6144, FD_ = 8192, EP_ = 10240, FL_BYTES = 10368;
constexpr size_t STAMP_OFF = FL_OFF + 16384;

constexpr unsigned RING = 0, XST = 81920, SCR = 147456, LDS1 = 163840;

DI unsigned cvtpk(float lo, float hi) { f32x2 v = {lo, hi}; bf2_t b = __builtin_convertvector(v, bf2_t); return __builtin_bit_cast(unsigned, b); }
DI float bf2f(unsigned v) { return __uint_as_float(v << 16); }
DI float rbf(float x) { return __uint_as_float(cvtpk(x, 0.f) << 16); }
DI s16x4 trr(unsigned a) { return __builtin_amdgcn_ds_read_tr16_b64_v4i16((LAS s16x4*)(size_t)a); }
DI bf16x8 ldsr(unsigned a) { return *(const LAS bf16x8*)(size_t)a; }
DI bf16x8 cat(s16x4 a, s16x4 b) { return __builtin_shufflevector(a, b, 0, 1, 2, 3, 4, 5, 6, 7); }
template <int IMM> DI void dma(unsigned voff, RSRC rs, unsigned soff) {
  asm volatile("buffer_load_dwordx4 %0, %1, %2 offen offset:%3 lds" ::"v"(voff), "s"(rs), "s"(soff), "n"(IMM) : "memory");
}
template <int IMM> DI void dma2(unsigned voff, RSRC rs, unsigned s0, unsigned s1) {
  unsigned t;
  asm volatile("s_add_u32 %0, %3, %4\n\tbuffer_load_dwordx4 %1, %2, %0 offen offset:%5 lds" : "=&s"(t) : "v"(voff), "s"(rs), "s"(s0), "s"(s1), "n"(IMM) : "memory", "scc");
}
template <int IMM, int ADD> DI void dma2i(unsigned voff, RSRC rs, unsigned s0) {
  unsigned t;
  asm volatile("s_add_u32 %0, %3, %4\n\tbuffer_load_dwordx4 %1, %2, %0 offen offset:%5 lds" : "=&s"(t) : "v"(voff), "s"(rs), "s"(s0), "n"(ADD), "n"(IMM) : "memory", "scc");
}
template <int IMM> DI void setm0(unsigned v) { asm volatile("s_add_u32 m0, %0, %1\n\ts_nop 0" ::"s"(v), "n"(IMM) : "memory", "scc"); }
#define PINS(x) asm volatile("" : "+s"(x))
template <int N> DI void wait_bar() { asm volatile("s_waitcnt vmcnt(%0)\n\ts_barrier" ::"n"(N) : "memory"); }
#define BST(slot)
template <int N> DI void wait_vm() { asm volatile("s_waitcnt vmcnt(%0)" ::"n"(N) : "memory"); }
DI RSRC mkrs(const void* p, unsigned bytes) { return __builtin_amdgcn_make_buffer_rsrc((void*)p, 0, bytes, 0x00020000); }
DI void st128(u32x4 v, RSRC r, unsigned vo, unsigned so) { __builtin_amdgcn_raw_buffer_store_b128(v, r, vo, so, SC1); }
DI void st64(u32x2 v, RSRC r, unsigned vo) { __builtin_amdgcn_raw_buffer_store_b64(v, r, vo, 0, SC1); }
DI u32x2 ld64_async(RSRC r, unsigned vo) { u32x2 v; asm volatile("buffer_load_dwordx2 %0, %1, %2, 0 offen sc1" : "=v"(v) : "v"(vo), "s"(r) : "memory"); return v; }
DI u32x2 ld64_sync(RSRC r, unsigned vo) { u32x2 v; asm volatile("buffer_load_dwordx2 %0, %1, %2, 0 offen sc1\n\ts_waitcnt vmcnt(0)" : "=&v"(v) : "v"(vo), "s"(r) : "memory"); return v; }
#define PINV(x) asm volatile("" : "+v"(x))
#define PINA(x) asm volatile("" : "+a"(x))
#define MFMA16(a, b, c) __builtin_amdgcn_mfma_f32_16x16x32_bf16((a), (b), (c), 0, 0, 0)
DI void mf_xa0(f32x16& d, const bf16x8& x, const bf16x8& w) { asm volatile("v_mfma_f32_32x32x16_bf16 %0, %1, %2, 0" : "=&v"(d) : "a"(x), "v"(w)); }
DI void mf_xa(f32x16& d, const bf16x8& x, const bf16x8& w) { asm volatile("v_mfma_f32_32x32x16_bf16 %0, %1, %2, %0" : "+v"(d) : "a"(x), "v"(w)); }
DI void mf_xb0(f32x16& d, const bf16x8& w, const bf16x8& x) { asm volatile("v_mfma_f32_32x32x16_bf16 %0, %1, %2, 0" : "=&v"(d) : "v"(w), "a"(x)); }
DI void mf_xb(f32x16& d, const bf16x8& w, const bf16x8& x) { asm volatile("v_mfma_f32_32x32x16_bf16 %0, %1, %2, %0" : "+v"(d) : "v"(w), "a"(x)); }
DI void mfma_done(f32x16& a, f32x16& b) { asm volatile("s_nop 7\n\ts_nop 7" : "+v"(a), "+v"(b)); }
DI void mfma_done4(f32x16 (&a)[4]) { asm volatile("s_nop 7\n\ts_nop 7" : "+v"(a[0]), "+v"(a[1]), "+v"(a[2]), "+v"(a[3])); }
#define STAMP(i) do {} while (0)

template <int V> using IC = std::integral_constant<int, V>;
template <class Fn, int... Is> DI void sfor_(Fn&& f, std::integer_sequence<int, Is...>) { (f(IC<Is>{}), ...); }
template <int N, class Fn> DI void sfor(Fn&& f) { sfor_(f, std::make_integer_sequence<int, N>{}); }

struct Args {
  const u16 *x, *lre, *lim, *bre, *bim, *cre, *cim, *ldt, *dsk, *w1, *w3, *w2, *gw;
  char* ws;
  float* out;
};

struct Stream {
  RSRC ra, rb;
  unsigned v0, v1, v2, v3;
  unsigned k, end, adv, lim, d1, d2, d3;
};
template <int Q> DI void st_piece(const Stream& s) {
  if constexpr (Q == 0) dma<0>(s.v0, s.ra, s.k);
  else if constexpr (Q == 1) dma2<1024>(s.v1, s.ra, s.k, s.d1);
  else if constexpr (Q == 2) dma2<2048>(s.v2, s.rb, s.k, s.d2);
  else dma2<3072>(s.v3, s.rb, s.k, s.d3);
}
DI void st_adv(Stream& s) { PINS(s.k); const unsigned k = s.k + s.adv; s.k = (k >= s.end) ? k - s.lim : k; }

struct Ctx {
  RSRC rx, rB, rw1, rw3, rnull;
  unsigned vS0, vS1, vS2, vS3, vD;
  unsigned lds0, m0ring;
  unsigned rho, w;
};
DI void stream_S(Stream& s, const Ctx& c, int i) {
  s.ra = c.rB; s.rb = c.rB; s.v0 = c.vS0; s.v1 = c.vS1; s.v2 = c.vS2; s.v3 = c.vS3;
  const unsigned base = (unsigned)(32 * i + 16 * (c.w & 1)) * 1280u;
  s.k = base + c.rho * 256u; s.end = base + 1280u; s.adv = 256u; s.lim = 1280u;
  s.d1 = 4096u; s.d2 = 8192u; s.d3 = 12288u;
}
DI void stream_D(Stream& s, const Ctx& c, int j) {
  s.ra = c.rw1; s.rb = c.rw3; s.v0 = s.v1 = s.v2 = s.v3 = c.vD;
  s.k = (unsigned)j * 128u + c.rho * (128u * 3456u); s.end = 640u * 3456u; s.adv = 64u * 3456u; s.lim = 640u * 3456u;
  s.d1 = 8u * 3456u - 1024u; s.d2 = 0u; s.d3 = 8u * 3456u - 1024u;
}
DI void stream_null(Stream& s, const Ctx& c) {
  s.ra = c.rnull; s.rb = c.rnull; s.k = 0; s.end = 1; s.adv = 0; s.lim = 0; s.d1 = s.d2 = s.d3 = 0;
}

struct Par { float lr, li, cr, ci, mr, mi; };
DI void csq(float& a, float& b) { const float r = a * a - b * b, i = 2.f * a * b; a = r; b = i; }
DI Par ssm_par(unsigned ulre, unsigned ulim, unsigned uldt) {
  const float Lr = bf2f(ulre), Li = bf2f(ulim);
  const float dt = rbf(expf(bf2f(uldt)));
  const float e = expf(dt * Lr), th = dt * Li;
  const float n = __builtin_rintf(th * 0.636619772f);
  float y = fmaf(n, -1.57079601287841796875f, th);
  y = fmaf(n, -3.1391647326017846353352069854736328125e-7f, y);
  y = fmaf(n, -5.390302529957764765544681040410068817436695098876953125e-15f, y);
  const float y2 = y * y;
  float ps = fmaf(y2, 2.6083159809786593541503e-06f, -0.0001981069071916863322258f);
  ps = fmaf(ps, y2, 0.00833307858556509017944336f); ps = fmaf(ps, y2, -0.166666597127914428710938f);
  const float sn = fmaf(ps * y2, y, y);
  float pc = fmaf(y2, -2.71811842367242206819355e-07f, 2.47990446951007470488548e-05f);
  pc = fmaf(pc, y2, -0.00138888787478208541870117f); pc = fmaf(pc, y2, 0.0416666641831398010253906f); pc = fmaf(pc, y2, -0.5f);
  const float cs = fmaf(pc, y2, 1.f);
  const int q = (int)n;
  float sv = (q & 1) ? cs : sn, cv = (q & 1) ? sn : cs;
  sv = (q & 2) ? -sv : sv; cv = ((q + 1) & 2) ? -cv : cv;
  Par r; r.lr = e * cv; r.li = e * sv;
  const float nr = r.lr - 1.f, ni = r.li, den = Lr * Lr + Li * Li;
  r.cr = 2.f * ((nr * Lr + ni * Li) / den); r.ci = 2.f * ((ni * Lr - nr * Li) / den);
  r.mr = r.lr; r.mi = r.li;
  csq(r.mr, r.mi); csq(r.mr, r.mi); csq(r.mr, r.mi); csq(r.mr, r.mi);
  return r;
}
DI void pin_par(Par& p) { asm volatile("" : "+v"(p.lr), "+v"(p.li), "+v"(p.cr), "+v"(p.ci), "+v"(p.mr), "+v"(p.mi)); }
DI unsigned ldu16(const u16* p, unsigned vo) { unsigned r; asm volatile("global_load_ushort %0, %1, %2" : "=v"(r) : "v"(vo), "s"(p) : "memory"); return r; }
DI float o_mul(float a, float b) { float r; asm volatile("v_mul_f32 %0, %1, %2" : "=v"(r) : "v"(a), "v"(b)); return r; }
DI float o_mulk(float a) { float r; asm volatile("v_mul_f32 %0, 0xbfb8aa3b, %1" : "=v"(r) : "v"(a)); return r; }
DI float o_add1(float a) { float r; asm volatile("v_add_f32 %0, 1.0, %1" : "=v"(r) : "v"(a)); return r; }
DI float o_exp(float a) { float r; asm volatile("v_exp_f32 %0, %1" : "=v"(r) : "v"(a)); return r; }
DI float o_rcp(float a) { float r; asm volatile("v_rcp_f32 %0, %1" : "=v"(r) : "v"(a)); return r; }
DI float o_fma(float a, float b, float c) { float r; asm volatile("v_fma_f32 %0, %1, %2, %3" : "=v"(r) : "v"(a), "v"(b), "v"(c)); return r; }
DI float o_fnma(float a, float b, float c) { float r; asm volatile("v_fma_f32 %0, -%1, %2, %3" : "=v"(r) : "v"(a), "v"(b), "v"(c)); return r; }
DI float o_fms(float a, float b, float c) { float r; asm volatile("v_fma_f32 %0, %1, %2, -%3" : "=v"(r) : "v"(a), "v"(b), "v"(c)); return r; }
DI float o_add(float a, float b) { float r; asm volatile("v_add_f32 %0, %1, %2" : "=v"(r) : "v"(a), "v"(b)); return r; }
DI unsigned o_cvt(float a, float b) { unsigned r; asm volatile("v_cvt_pk_bf16_f32 %0, %1, %2" : "=v"(r) : "v"(a), "v"(b)); return r; }
DI void cmad(float& a, float& b, float mr, float mi, float u, float v) {
  const float x = fmaf(mr, a, fmaf(-mi, b, u)), y = fmaf(mr, b, fmaf(mi, a, v));
  a = x; b = y;
}

template <class Next, class Bar, class Gap>
DI void s_body(const bf16x8 (&xf)[40], f32x16& zre, f32x16& zim, Stream& st, const Ctx& c, const unsigned (&sa)[8], Next next, Bar bar, Gap gap) {
  bf16x8 wr[3], wi[3];
  auto rd = [&](int ks, int im) -> bf16x8 {
    const int n = ks >> 3, s = ks & 7;
    return n < 4 ? ldsr(sa[s] + n * 16384 + im * 8192) : ldsr(sa[s] + 65536u + im * 8192);
  };
  BST(20); wait_bar<8>(); BST(21);
  setm0<3 * 16384>(c.m0ring);
  wr[0] = rd(0, 0); wi[0] = rd(0, 1); wr[1] = rd(1, 0); wi[1] = rd(1, 1);
  st_piece<0>(st);
  SB();
  sfor<80>([&](auto GG) {
    constexpr int G = GG, ks = G >> 1, im = G & 1;
    if constexpr (im == 0 && (ks & 7) == 6 && ks < 38) {
      constexpr int n = (ks >> 3) + 1;
      BST(20 + 2 * n); wait_bar<8>(); BST(21 + 2 * n);
      bar(IC<n>{});
      if constexpr (n == 2) next(st);
      setm0<((n + 3) % 5) * 16384>(c.m0ring);
      SB();
    }
    if constexpr (ks == 0) { if constexpr (im) mf_xa0(zim, xf[0], wi[0]); else mf_xa0(zre, xf[0], wr[0]); }
    else { if constexpr (im) mf_xa(zim, xf[ks], wi[ks % 3]); else mf_xa(zre, xf[ks], wr[ks % 3]); }
    SB();
    if constexpr (im == 0 && (ks & 1) == 0 && ks <= 36) {
      constexpr int q = ((ks + 2) >> 1) & 3;
      st_piece<q>(st);
      if constexpr (q == 3) st_adv(st);
    }
    if constexpr (ks + 2 < 40) { if constexpr (im) wi[(ks + 2) % 3] = rd(ks + 2, 1); else wr[(ks + 2) % 3] = rd(ks + 2, 0); }
    gap(GG);
    SB();
  });
  mfma_done(zre, zim);
}

template <bool FIRST, int W8, int W9, class Next, class Bar, class Gap, class Pre>
DI void d_body(bf16x8 (&xf)[40], f32x16 (&acc)[4], Stream& st, const Ctx& c, const unsigned (&rbd)[4], const unsigned (&sa)[8], unsigned xso,
               Next next, Bar bar, Gap gap, Pre pre) {
  s16x4 wl[2][4], wh[2][4];
  auto rd1 = [&](int ks, int t, int half) -> s16x4 {
    const unsigned off = ((ks >> 2) % 5) * 16384 + (ks & 3) * 4096 + (t >> 1) * 2048 + half * 512;
    return trr(off >= 49152 ? rbd[2 + (t & 1)] + (off - 49152) : rbd[t & 1] + off);
  };
  const unsigned xdelta = XST + c.w * 16384u;
  unsigned xk = xso + c.rho * 256u;
  const unsigned xm0 = c.lds0 + xdelta;
  auto xpiece = [&](auto M, auto Q) {
    constexpr int m = M, q = Q;
    if constexpr (q == 0 && m > 0) { PINS(xk); xk += 256u; xk = xk >= xso + 1280u ? xk - 1280u : xk; }
    if constexpr ((q & 3) == 0) setm0<(m & 1) * 8192 + (q >> 2) * 4096>(xm0);
    constexpr int ro = (16 * (q & 1) + 4 * (q >> 1)) * 1280 - 1024 * (q & 3);
    dma2i<1024 * (q & 3), ro>((q & 3) == 0 ? c.vS0 : ((q & 3) == 1 ? c.vS1 : ((q & 3) == 2 ? c.vS2 : c.vS3)), c.rx, xk);
  };
  auto wpiece = [&](auto N, auto Q) {
    constexpr int n = N, q = Q;
    if constexpr (q == 0) { if constexpr (n == 10) next(st); setm0<(n % 5) * 16384>(c.m0ring); }
    st_piece<q>(st);
    if constexpr (q == 3) st_adv(st);
  };
  if constexpr (FIRST) {
    sfor<8>([&](auto Q) { xpiece(IC<0>{}, Q); });
    sfor<4>([&](auto Q) { wpiece(IC<0>{}, Q); });
    sfor<4>([&](auto Q) { wpiece(IC<1>{}, Q); });
    sfor<4>([&](auto Q) { xpiece(IC<1>{}, Q); });
    SB();
    pre(IC<-1>{});
    SB();
  }
  BST(FIRST ? 0 : 40); wait_bar<8>(); BST(FIRST ? 1 : 41);
  bar(IC<0>{});
  if constexpr (FIRST) {
#pragma unroll
    for (int s = 0; s < 8; ++s) xf[s] = ldsr(sa[s] + xdelta);
    SB();
  } else setm0<3 * 16384>(c.m0ring);
#pragma unroll
  for (int t = 0; t < 4; ++t) { wl[0][t] = rd1(0, t, 0); wh[0][t] = rd1(0, t, 1); }
  if constexpr (!FIRST) st_piece<0>(st);
  SB();
  sfor<160>([&](auto GG) {
    constexpr int G = GG, ks = G >> 2, t = G & 3;
    if constexpr (t == 0 && (ks & 3) == 3 && ks < 39) {
      constexpr int n = (ks >> 2) + 1;
      BST((FIRST ? 0 : 40) + 2 * n);
      wait_bar<FIRST ? ((n & 1) ? 10 : 6) : (n == 8 ? W8 : (n == 9 ? W9 : 8))>();
      BST((FIRST ? 1 : 41) + 2 * n);
      bar(IC<n>{});
      if constexpr (!FIRST && n < 7) setm0<((n + 3) % 5) * 16384>(c.m0ring);
      SB();
    }
    const bf16x8 wf = cat(wl[ks & 1][t], wh[ks & 1][t]);
    if constexpr (ks == 0) mf_xb0(acc[t], wf, xf[0]); else mf_xb(acc[t], wf, xf[ks]);
    SB();
    if constexpr (FIRST) {
      if constexpr ((t & 1) == 0) {
        constexpr int s = G >> 1;
        if constexpr (s < 4) xpiece(IC<1>{}, IC<4 + s>{});
        else if constexpr (s < 60) {
          constexpr int u = (s - 4) % 16, m = (s - 4) / 16;
          if constexpr (u < 8) wpiece(IC<2 + 2 * m + (u >> 2)>{}, IC<u & 3>{}); else xpiece(IC<2 + m>{}, IC<u - 8>{});
        } else if constexpr (s < 68) wpiece(IC<10 + ((s - 60) >> 2)>{}, IC<s & 3>{});
        else if constexpr (s < 70) pre(IC<s - 68>{});
        else if constexpr (s < 74) wpiece(IC<12>{}, IC<s - 70>{});
        else if constexpr (s < 79) pre(IC<s - 72>{});
      }
      constexpr int g8 = G - 28;
      if constexpr (g8 >= 0 && (g8 & 31) < 8 && (g8 >> 5) < 4) xf[8 * ((g8 >> 5) + 1) + (g8 & 7)] = ldsr(sa[g8 & 7] + xdelta + (((g8 >> 5) + 1) & 1) * 8192);
    } else if constexpr (t == 0 && ks < 27) {
      st_piece<(ks + 1) & 3>(st);
      if constexpr (((ks + 1) & 3) == 3) st_adv(st);
    }
    if constexpr (ks < 39) { wl[(ks + 1) & 1][t] = rd1(ks + 1, t, 0); wh[(ks + 1) & 1][t] = rd1(ks + 1, t, 1); }
    gap(GG);
    SB();
  });
  mfma_done4(acc);
}

__global__ __launch_bounds__(256, 1) void fused(Args p) {
  extern __shared__ __attribute__((aligned(16))) char smem[];
  const int lane = threadIdx.x & 63;
  const unsigned w = __builtin_amdgcn_readfirstlane(threadIdx.x >> 6);
  const int bid = blockIdx.x, xcd = bid & 7, r = bid >> 3, idx = r >> 2;
  const int rb = (xcd >> 1) * 4 + (r & 3), cg = 2 * idx + (xcd & 1);
  STAMP(0);
  Ctx c;
  c.w = w; c.rho =
    (unsigned)(rb + idx) % 3u * 2u;
  c.lds0 = (unsigned)(size_t)smem; c.m0ring = c.lds0 + RING + w * 4096u;
  BST(62);
  const int l32 = lane & 31, h = lane >> 5;
  const bool heavy = cg < 11, two = cg >= 11 && cg < 15;
  const int blk1 = two ? cg + 5 : cg;
  unsigned cpf;
  {
    u64 pc; unsigned len;
    asm volatile("s_getpc_b64 %0\n1:\n\ts_mov_b32 %1, fused_code_end-1b" : "=s"(pc), "=s"(len));
    const unsigned line = CPF_ALL ? (((r * 4 + w) & 7) * 64 + lane) : (r * 16 + w * 4 + (lane & 3));
    asm volatile("buffer_load_dword %0, %1, %2, 0 offen" : "=v"(cpf) : "v"((CPF_ALL || lane < 4) ? line * 128u : OOB), "s"(mkrs((const void*)pc, CPF ? len : 0)) : "memory");
  }
  unsigned raw[2][3];
#pragma unroll
  for (int i = 0; i < 2; ++i) {
    const unsigned ch = (32 * (i ? blk1 : cg) + l32) * 2;
    raw[i][0] = ldu16(p.lre, ch); raw[i][1] = ldu16(p.lim, ch); raw[i][2] = ldu16(p.ldt, ch);
  }
  c.rx = mkrs(p.x, L_ * D_ * 2); c.rB = mkrs(w < 2 ? p.bre : p.bim, P_ * D_ * 2);
  c.rw1 = mkrs(p.w1, D_ * F_ * 2); c.rw3 = mkrs((const char*)p.w3 - 2048, D_ * F_ * 2 + 2048); c.rnull = mkrs(p.x, 0);
  {
    const unsigned rr = lane >> 4, t = (lane & 15) ^ rr;
    c.vS0 = rr * 1280 + t * 16; c.vS1 = rr * 1280 + (t ^ 4) * 16; c.vS2 = rr * 1280 + (t ^ 8) * 16; c.vS3 = rr * 1280 + (t ^ 12) * 16;
    const unsigned lrow = lane >> 3, lch = (lane & 7) ^ (((lrow >> 1) & 1) << 2);
    c.vD = (lrow + 16 * w) * 3456 + lch * 16;
  }
  u64 ep = *(const __attribute__((address_space(4))) u64*)(size_t)(p.ws + FL_OFF + EP_);
  bf16x8 xf[40];
  Par par[2];
  Stream st;
  stream_D(st, c, cg);
  unsigned sa[8], rbd[4];
  const unsigned tb = c.lds0 + XST + w * 16384;
  int v = (rb * 16 + cg) * 4 + w, pc, ft, ntw, ci_, ct;
  bool hasw, hasc;

  f32x16 accp[4];
  {
    unsigned pv[6];
    d_body<true, 0, 0>(xf, accp, st, c, rbd, sa, (unsigned)(128 * rb + 32 * w) * 1280u,
      [&](Stream& s) { PINS(c.rho); stream_S(s, c, cg); },
      [&](auto) {},
      [&](auto GG) {
        constexpr int G = GG;
        if constexpr (G == 119) {
          PINS(v);
          pc = v >> 1; ft = pc >> 3; ntw = pc & 7; ci_ = v / 40; ct = v - 40 * ci_;
          hasw = !(v & 1) && pc < 432; hasc = v < 800;
        }
        if constexpr (G >= 121 && G < 131 && (G & 1)) {
          constexpr int i = (G - 121) >> 1;
          const unsigned o = 1024 * i + 16 * lane, row = o / 160;
          pv[i] = row * 1120 + o; PINV(pv[i]);
        }
        if constexpr (G == 131) { pv[5] = (unsigned)(16 * ct + (lane >> 2)) * 1280u + 64u * ci_ + 16u * (lane & 3); PINV(pv[5]); }
      },
      [&](auto I) {
        constexpr int i = I;
        if constexpr (i < 0) {
          int ln = lane; PINV(ln);
          const unsigned m32 = ln & 31, hh = ln >> 5;
#pragma unroll
          for (int s = 0; s < 8; ++s) { sa[s] = c.lds0 + m32 * 256 + (((2 * s + hh) ^ (m32 & 15)) << 4); PINV(sa[s]); }
          const unsigned blk = (ln >> 4) & 1, q = (ln & 15) >> 2, pp = ln & 3;
          const unsigned row = 8 * hh + q, ch = 2 * blk + (pp >> 1);
          const unsigned a = c.lds0 + row * 128 + ((ch ^ (((row >> 1) & 1) << 2)) * 16) + 8 * (pp & 1);
          rbd[0] = a; rbd[1] = a ^ 64; rbd[2] = a + 49152; rbd[3] = (a ^ 64) + 49152;
          asm volatile("s_waitcnt vmcnt(20)" : "+v"(raw[0][0]), "+v"(raw[0][1]), "+v"(raw[0][2]), "+v"(raw[1][0]), "+v"(raw[1][1]), "+v"(raw[1][2]), "+s"(ep), "+v"(cpf),
                       "+v"(rbd[0]), "+v"(rbd[1]), "+v"(rbd[2]), "+v"(rbd[3])::"memory");
          par[0] = ssm_par(raw[0][0], raw[0][1], raw[0][2]); pin_par(par[0]);
        } else {
          setm0<8192 + 1024 * i>(tb);
          if constexpr (i < 5) dma<0>(pv[i], mkrs(p.w2, hasw ? F_ * D_ * 2 : 0), (unsigned)(32 * ft) * 1280u + 160u * ntw);
          else dma<0>(pv[5], mkrs(i == 5 ? p.cre : p.cim, hasc ? D_ * P_ * 2 : 0), 0);
        }
      });
  }
  STAMP(1);
  const u64 tag64 = ep * 0x9E3779B97F4A7C15ull + 0x1234567ull;
  const u32x2 tag = {(unsigned)tag64, (unsigned)(tag64 >> 32)};
  const RSRC rfl = mkrs(p.ws + FL_OFF, FL_BYTES);
  const RSRC rst = mkrs(p.ws + ST_OFF, ST_BYTES);
  const RSRC rht = mkrs(p.ws + HT_OFF, HT_BYTES);
  const unsigned mtbase = (unsigned)((2 * rb + (w >> 1)) * KS2 * 4 + 2 * (w & 1)) * 1024u;
  const unsigned vlead = (w == 0 && lane == 0) ? 0u : OOB;
  const unsigned segw = c.lds0 + SCR + ((2 * w + h) * 32 + l32) * 8, segr = c.lds0 + SCR + l32 * 8;
  const unsigned vst = (unsigned)((l32 >> 4) * 1024 + h * 256 + (l32 & 15) * 16);
  const unsigned vo = lane * 16;

  f32x16 zr[2], zi[2];
  {
    float ta[3], tp[2], hv[2];
    unsigned pk[4];
    s16x4 t0[2], t1[2];
    u32x4 ca, cb2;
    const RSRC rbw = mkrs(p.ws + BT_OFF, hasw ? BT_BYTES : 0), rbc = mkrs(p.ws + BT_OFF, hasc ? BT_BYTES : 0);
    const unsigned sow = (unsigned)((ntw * KS2 + ft) * 5) * 1024u;
    const unsigned soc = (unsigned)(((ct / 5) * KS2 + 56 + 2 * ci_) * 5 + ct % 5) * 1024u;
    const unsigned sof = mtbase + (unsigned)(2 * cg) * 4096u;
    s_body(xf, zr[0], zi[0], st, c, sa,
      [&](Stream& s) { PINS(c.rho); if (heavy) stream_D(s, c, 16 + cg); else if (two) stream_S(s, c, cg + 5); else stream_null(s, c); },
      [&](auto) {},
      [&](auto GG) {
        constexpr int G = GG;
        constexpr int P = G / 5, ph = G % 5, a = 2 * P, b = a + 1;
        auto ga = [&](int e) { return accp[e >> 4][e & 15]; };
        auto up = [&](int e) { return accp[2 + (e >> 4)][e & 15]; };
        if constexpr (ph == 0) {
          ta[0] = o_mulk(ga(a)); ta[1] = o_mulk(ga(b));
          if constexpr (P > 0) { hv[1] = o_mul(tp[1], ta[2]); }
        }
        if constexpr (ph == 1) {
          ta[0] = o_exp(ta[0]);
          if constexpr (P > 0) {
            pk[(P - 1) & 3] = o_cvt(hv[0], hv[1]);
            if constexpr (((P - 1) & 3) == 3) st128((u32x4){pk[0], pk[1], pk[2], pk[3]}, rht, vst + (((P - 1) >> 2) & 1) * 512, sof + ((P - 1) >> 3) * 4096);
          }
        }
        if constexpr (ph == 2) { ta[1] = o_exp(ta[1]); ta[0] = o_add1(ta[0]); }
        if constexpr (ph == 3) { ta[0] = o_rcp(ta[0]); ta[1] = o_add1(ta[1]); tp[0] = o_mul(ga(a), up(a)); }
        if constexpr (ph == 4) { ta[2] = o_rcp(ta[1]); tp[1] = o_mul(ga(b), up(b)); hv[0] = o_mul(tp[0], ta[0]); }
        if constexpr (G == 79) {
          asm volatile("s_nop 0");
          hv[1] = o_mul(tp[1], ta[2]); pk[3] = o_cvt(hv[0], hv[1]);
          st128((u32x4){pk[0], pk[1], pk[2], pk[3]}, rht, vst + 512, sof + 4096);
        }
        if constexpr (G >= 30 && G < 60 && (G - 30) % 5 == 2 && (G - 30) / 5 < 5) {
          const unsigned q = lane >> 4, qq = (lane & 15) >> 2, pp = lane & 3;
          const unsigned ad = tb + 8192 + (16 * (q >> 1) + 4 * (q & 1) + qq) * 160 + 8 * pp + 32 * ((G - 30) / 5);
          t0[0] = trr(ad); t1[0] = trr(ad + 1280);
        }
        if constexpr (G >= 35 && G < 60 && (G - 35) % 5 == 0) st128(__builtin_bit_cast(u32x4, cat(t0[0], t1[0])), rbw, vo, sow + 1024 * ((G - 35) / 5));
        if constexpr (G == 62) {
          const unsigned ad = tb + 8192 + 5120 + (lane & 15) * 64 + (lane >> 4) * 16;
          ca = __builtin_bit_cast(u32x4, ldsr(ad)); cb2 = __builtin_bit_cast(u32x4, ldsr(ad + 1024));
        }
        if constexpr (G == 65 || G == 70) {
          constexpr int o = G == 65 ? 0 : 2;
          const unsigned a0 = ca[o], a1 = ca[o + 1], b0 = cb2[o] ^ 0x80008000u, b1 = cb2[o + 1] ^ 0x80008000u;
          st128((u32x4){__builtin_amdgcn_perm(b0, a0, 0x05040100u), __builtin_amdgcn_perm(b0, a0, 0x07060302u),
                        __builtin_amdgcn_perm(b1, a1, 0x05040100u), __builtin_amdgcn_perm(b1, a1, 0x07060302u)}, rbc, vo, soc + (o >> 1) * 5120);
        }
      });
  }
  STAMP(2);
  auto flag_ok = [&](u32x2 f, bool need) { return __all(!need || (f[0] == tag[0] && f[1] == tag[1])); };
  auto spin = [&](unsigned off, bool need) {
#pragma unroll 1
    for (unsigned spins = 0; spins < (1u << 20); ++spins) {
      if (flag_ok(ld64_sync(rfl, off), need)) break;
    }
  };
  auto spin_fa = [&]() {
#pragma unroll 1
    for (unsigned spins = 0; spins < (1u << 20); ++spins) {
      u32x2 f[4];
#pragma unroll
      for (int u = 0; u < 4; ++u) f[u] = ld64_async(rfl, FA_ + (64 * u + lane) * 8);
      asm volatile("s_waitcnt vmcnt(0)" : "+v"(f[0]), "+v"(f[1]), "+v"(f[2]), "+v"(f[3])::"memory");
      if (flag_ok(f[0], true) && flag_ok(f[1], true) && flag_ok(f[2], true) && flag_ok(f[3], true)) break;
    }
  };
  const int mt = 2 * rb + (idx & 1), nt = 4 * (xcd & 1) + (idx >> 1);
  const RSRC ra = mkrs(p.ws + HT_OFF + (size_t)mt * KS2 * 4096, KS2 * 4096);
  const RSRC rbb = mkrs(p.ws + BT_OFF + (size_t)nt * KS2 * 5120, KS2 * 5120);
  constexpr int NS = 24, DEP = 5, PRE = PRE_T + PRE_E;
  bf16x8 fa[DEP][4], fb[DEP][5];
  const unsigned voz = w < 2 ? vo : OOB;
  auto ld = [&](int s, int g) {
    const unsigned ks = w + 4 * s, va = s == 13 ? voz : vo;
    if (g < 4) fa[s % DEP][g] = __builtin_bit_cast(bf16x8, __builtin_amdgcn_raw_buffer_load_b128(ra, va + g * 1024, ks * 4096, SC1));
    else if (g < 8) fb[s % DEP][g - 4] = __builtin_bit_cast(bf16x8, __builtin_amdgcn_raw_buffer_load_b128(rbb, va + (g - 4) * 1024, ks * 5120, SC1));
    else fb[s % DEP][4] = __builtin_bit_cast(bf16x8, __builtin_amdgcn_raw_buffer_load_b128(rbb, va, ks * 5120 + 4096, SC1));
  };
  unsigned tch[2];
  auto touch = [&](int i) {
    if (i == 0) tch[0] = __builtin_amdgcn_raw_buffer_load_b32(ra, lane * 128, ((idx >> 1) * 4 + w) * 8192, SC1);
    else tch[1] = __builtin_amdgcn_raw_buffer_load_b32(rbb, lane * 128, 10240 + ((((r & 3) * 2 + (idx & 1)) * 4 + w) * 8192), SC1);
  };
  const unsigned fs_off = FS_ + (cg * 16 + (lane & 15)) * 8;
  auto st_off = [&](int blk, int u) { const int j = rb - 1 - (8 * h + u); return j >= 0 ? (unsigned)(j * 640 + 32 * blk + l32) * 8u : OOB; };
  const unsigned cb = (l32 >> 2) ^ (2 * h);
  auto tw_ad = [&](int u) { return tb + h * 512 + ((cb ^ ((u & 1) + 4 * (u >> 1))) << 4) + (l32 & 3) * 4; };
  auto tr_ad = [&](int rt, int kk) { const unsigned row = lane & 15, q = lane >> 4; return tb + (16 * rt + row) * 128 + (((2 * q + kk) ^ ((row >> 1) & 7)) << 4); };
  u32x2 sv[8];
  float nr, ni, qr, qi;
  zr[1] = zr[0]; zi[1] = zi[0]; par[1] = par[0];

  if (heavy) {
    f32x16 acc[4];
    float sr, si, u0, u1;
    f32x2 sg[8];
    u32x2 fa_[4], fsv;
    d_body<false, 5 + 2 * TOUCH + (14 * PRE_T + 49) / 50, 2 * TOUCH + (30 * PRE_T + 49) / 50>(xf, acc, st, c, rbd, sa, 0u, [&](Stream&) {},
      [&](auto NN) {
        constexpr int n = NN;
        if constexpr (n == 2) st64(tag, rfl, vlead + FA_ + (rb * 16 + cg) * 8);
        if constexpr (n == 6) st64(tag, rfl, vlead + FS_ + (cg * 16 + rb) * 8);
        if constexpr (n == 7) {
          asm volatile("s_waitcnt vmcnt(6)" : "+v"(fa_[0]), "+v"(fa_[1]), "+v"(fa_[2]), "+v"(fa_[3])::"memory");
          if (!(flag_ok(fa_[0], true) && flag_ok(fa_[1], true) && flag_ok(fa_[2], true) && flag_ok(fa_[3], true))) spin_fa();
          asm volatile("" ::: "memory");
        }
      },
      [&](auto GG) {
        constexpr int G = GG;
        if constexpr (G == 0) { sr = zr[0][0]; si = zi[0][0]; }
        if constexpr (G < 30) {
          constexpr int j = (G >> 1) + 1;
          if constexpr ((G & 1) == 0) { u0 = o_fnma(par[0].li, si, zr[0][j]); u1 = o_fma(par[0].li, sr, zi[0][j]); }
          else { sr = o_fma(par[0].lr, sr, u0); si = o_fma(par[0].lr, si, u1); }
        }
        if constexpr (G == 30) *(LAS f32x2*)(size_t)segw = (f32x2){sr, si};
        if constexpr (G >= 32 && G < 44) {
          if constexpr (G == 32) { nr = par[0].mr; ni = par[0].mi; }
          if constexpr (G == 38) { qr = nr; qi = ni; }
          float& a = G < 38 ? nr : qr; float& b = G < 38 ? ni : qi;
          if constexpr ((G & 1) == 0) { u0 = o_mul(b, b); u1 = o_mul(a, b); }
          else { a = o_fms(a, a, u0); b = o_add(u1, u1); }
        }
        if constexpr (G >= 44 && G < 48) { sg[2 * (G - 44)] = *(const LAS f32x2*)(size_t)(segr + 512 * (G - 44)); sg[2 * (G - 44) + 1] = *(const LAS f32x2*)(size_t)(segr + 512 * (G - 44) + 256); }
        if constexpr (G == 46) { sr = sg[0][0]; si = sg[0][1]; }
        if constexpr (G >= 46 && G < 60) {
          constexpr int j = ((G - 46) >> 1) + 1;
          if constexpr ((G & 1) == 0) { u0 = o_fnma(par[0].mi, si, sg[j][0]); u1 = o_fma(par[0].mi, sr, sg[j][1]); }
          else { sr = o_fma(par[0].mr, sr, u0); si = o_fma(par[0].mr, si, u1); }
        }
        if constexpr (G == 59) st64((u32x2){__float_as_uint(sr), __float_as_uint(si)}, rst, (w == 0 && h == 0) ? (unsigned)(rb * 640 + 32 * cg + l32) * 8u : OOB);
        if constexpr (G >= 84 && G < 88) fa_[G - 84] = ld64_async(rfl, FA_ + (64 * (G - 84) + lane) * 8);
        if constexpr (TOUCH && (G == 108 || G == 109)) touch(G - 108);
        if constexpr (G >= 110 && PRE_T > 0) { constexpr int i = ((G - 110) * PRE_T + 49) / 50; if constexpr (i < PRE_T && (50 * i) / (PRE_T ? PRE_T : 1) == G - 110) ld(i / 9, i % 9); }
        if constexpr (G == 141) fsv = __builtin_amdgcn_raw_buffer_load_b64(rfl, fs_off, 0, SC1);
      },
      [&](auto) {});
    STAMP(3);
    if (!flag_ok(fsv, (lane & 15) < rb)) spin(fs_off, (lane & 15) < rb);
    asm volatile("" ::: "memory");
#pragma unroll
    for (int u = 0; u < 8; ++u) sv[u] = __builtin_amdgcn_raw_buffer_load_b64(rst, st_off(cg, u), 0, SC1);
    SB();
    const unsigned sof = mtbase + (unsigned)(32 + 2 * cg) * 4096u;
    float e[8];
    sfor<12>([&](auto HH) {
      constexpr int H = HH / 3, ph = HH % 3, t = H >> 1, o = 8 * (H & 1);
      if constexpr (ph == 0) {
#pragma unroll
        for (int u = 0; u < 8; ++u) e[u] = __builtin_amdgcn_exp2f(acc[t][o + u] * -1.44269504f);
      } else if constexpr (ph == 1) {
#pragma unroll
        for (int u = 0; u < 8; ++u) e[u] = __builtin_amdgcn_rcpf(1.f + e[u]);
      } else {
        unsigned pk[4];
#pragma unroll
        for (int u = 0; u < 8; u += 2) pk[u >> 1] = cvtpk(acc[t][o + u] * acc[2 + t][o + u] * e[u], acc[t][o + u + 1] * acc[2 + t][o + u + 1] * e[u + 1]);
        st128((u32x4){pk[0], pk[1], pk[2], pk[3]}, rht, vst + (H & 1) * 512, sof + t * 4096);
      }
#pragma unroll
      for (int i = PRE_T + HH * PRE_E / 12; i < PRE_T + (HH + 1) * PRE_E / 12; ++i) ld(i / 9, i % 9);
      SB();
    });
  } else {
    if (two) s_body(xf, zr[1], zi[1], st, c, sa, [&](Stream& s) { stream_null(s, c); },
                    [&](auto NN) { if constexpr (NN == 2) st64(tag, rfl, vlead + FA_ + (rb * 16 + cg) * 8); },
                    [&](auto GG) { if constexpr (GG == 4) { par[1] = ssm_par(raw[1][0], raw[1][1], raw[1][2]); pin_par(par[1]); } });
    else {
      setm0<0>(tb); dma<0>(vo, mkrs(p.gw, 1280), 0); dma<1024>(vo, mkrs(p.gw, 1280), 0);
      wait_bar<2>();
      st64(tag, rfl, vlead + FA_ + (rb * 16 + cg) * 8);
    }
    STAMP(3);
    BST(32);
    float sr[2], si[2], u0[2], u1[2];
#pragma unroll
    for (int i = 0; i < 2; ++i) { sr[i] = zr[i][0]; si[i] = zi[i][0]; }
#pragma unroll
    for (int j = 1; j < 16; ++j) {
#pragma unroll
      for (int i = 0; i < 2; ++i) { u0[i] = o_fnma(par[i].li, si[i], zr[i][j]); u1[i] = o_fma(par[i].li, sr[i], zi[i][j]); }
#pragma unroll
      for (int i = 0; i < 2; ++i) { sr[i] = o_fma(par[i].lr, sr[i], u0[i]); si[i] = o_fma(par[i].lr, si[i], u1[i]); }
    }
#pragma unroll
    for (int i = 0; i < 2; ++i) *(LAS f32x2*)(size_t)(segw + i * 2048) = (f32x2){sr[i], si[i]};
    asm volatile("s_waitcnt lgkmcnt(0)\n\ts_barrier" ::: "memory");
    f32x2 sg[2][8];
#pragma unroll
    for (int g = 0; g < 8; ++g)
#pragma unroll
      for (int i = 0; i < 2; ++i) sg[i][g] = *(const LAS f32x2*)(size_t)(segr + i * 2048 + g * 256);
    SB();
#pragma unroll
    for (int i = 0; i < 2; ++i) { sr[i] = sg[i][0][0]; si[i] = sg[i][0][1]; }
#pragma unroll
    for (int g = 1; g < 8; ++g) {
#pragma unroll
      for (int i = 0; i < 2; ++i) { u0[i] = o_fnma(par[i].mi, si[i], sg[i][g][0]); u1[i] = o_fma(par[i].mi, sr[i], sg[i][g][1]); }
#pragma unroll
      for (int i = 0; i < 2; ++i) { sr[i] = o_fma(par[i].mr, sr[i], u0[i]); si[i] = o_fma(par[i].mr, si[i], u1[i]); }
    }
#pragma unroll
    for (int i = 0; i < 2; ++i)
      st64((u32x2){__float_as_uint(sr[i]), __float_as_uint(si[i])}, rst, (w == 0 && h == 0) ? (unsigned)(rb * 640 + 32 * (i ? blk1 : cg) + l32) * 8u : OOB);
    u32x2 fa_[4];
#pragma unroll
    for (int u = 0; u < 4; ++u) fa_[u] = ld64_async(rfl, FA_ + (64 * u + lane) * 8);
    nr = par[0].mr; ni = par[0].mi; csq(nr, ni); csq(nr, ni); csq(nr, ni);
    qr = nr; qi = ni; csq(qr, qi); csq(qr, qi); csq(qr, qi);
    BST(33);
    asm volatile("s_waitcnt vmcnt(0)" : "+v"(fa_[0]), "+v"(fa_[1]), "+v"(fa_[2]), "+v"(fa_[3]), "+v"(nr), "+v"(ni), "+v"(qr), "+v"(qi)::"memory");
    st64(tag, rfl, vlead + FS_ + (cg * 16 + rb) * 8);
    BST(34);
    if (!two) {
      f32x16 a;
      unsigned gk = c.rho * 256u;
#pragma unroll
      for (int n = 0; n < 5; ++n) {
        const unsigned ad = tb + gk + 16 * h;
        bf16x8 gf[8];
#pragma unroll
        for (int s = 0; s < 8; ++s) gf[s] = ldsr(ad + 32 * s);
#pragma unroll
        for (int s = 0; s < 8; ++s) { if (n + s == 0) mf_xb0(a, gf[s], xf[0]); else mf_xb(a, gf[s], xf[8 * n + s]); }
        gk = gk == 1024u ? 0u : gk + 256u;
      }
      asm volatile("s_nop 7\n\ts_nop 7" : "+v"(a));
      const float lg = rbf(a[0]);
      const float g = rbf(1.f / (1.f + __expf(-lg)));
      __builtin_amdgcn_raw_buffer_store_b32(__float_as_uint(g), mkrs(p.ws + G_OFF, G_BYTES), h == 0 ? (unsigned)(128 * rb + 32 * w + l32) * 4u : OOB, 0, SC1);
    }
    BST(35);
    if (!(flag_ok(fa_[0], true) && flag_ok(fa_[1], true) && flag_ok(fa_[2], true) && flag_ok(fa_[3], true))) spin_fa();
    asm volatile("" ::: "memory");
    BST(36);
    if (TOUCH) { touch(0); touch(1); }
#pragma unroll
    for (int g = 0; g < PRE - 9; ++g) ld(g / 9, g % 9);
    const u32x2 fsv = __builtin_amdgcn_raw_buffer_load_b64(rfl, fs_off, 0, SC1);
#pragma unroll
    for (int g = PRE - 9; g < PRE; ++g) ld(g / 9, g % 9);
    BST(37);
    if (!flag_ok(fsv, (lane & 15) < rb)) spin(fs_off, (lane & 15) < rb);
    asm volatile("" ::: "memory");
#pragma unroll
    for (int u = 0; u < 8; ++u) sv[u] = __builtin_amdgcn_raw_buffer_load_b64(rst, st_off(cg, u), 0, SC1);
  }
  STAMP(5);
  BST(60);

  {
    f32x4 acc[4][5], accS[4][5];
    u32x2 dv[5], fz;
    float gv[4], gm[5];
    f32x2 sg[7];
    float cr, ci, u0, u1;
    bf16x8 tf[4];
    unsigned twa[4], orow[5];
    auto tau = [](int i) { return (i & 32) + 16 * ((i >> 2) & 1) + 4 * ((i >> 3) & 3) + (i & 3); };
    SB();
    sfor<NS * 10>([&](auto QQ) {
      constexpr int Q = QQ, s = Q / 10, g = Q % 10;
      constexpr int R_ = 216 - PRE, n1 = PRE + (Q * R_) / 220, n0 = Q ? PRE + ((Q - 1) * R_) / 220 : PRE - 1;
      constexpr bool go = n1 > n0 && n1 < 216;
      if constexpr (g == 0) {
        BST(64 + s);
        if constexpr (s == 2) {
          wait_bar<16>();
          st64(tag, rfl, vlead + FC_ + (rb * 16 + cg) * 8);
          if (TOUCH) asm volatile("" ::"v"(tch[0]), "v"(tch[1]));
        }
        if constexpr (s == 8) {
          wait_bar<16>();
          st64(tag, rfl, vlead + FB_ + (rb * 16 + cg) * 8);
        }
        SB();
      }
      if constexpr (Q == 90) {
        if (two) {
          u32x2 sw[8];
          f32x2 s2[7];
#pragma unroll
          for (int u = 0; u < 8; ++u) sw[u] = __builtin_amdgcn_raw_buffer_load_b64(rst, st_off(blk1, u), 0, SC1);
#pragma unroll
          for (int u = 0; u < 7; ++u) s2[u] = *(const LAS f32x2*)(size_t)(segr + 2048 + u * 256);
          nr = par[1].mr; ni = par[1].mi; csq(nr, ni); csq(nr, ni); csq(nr, ni);
          qr = nr; qi = ni; csq(qr, qi); csq(qr, qi); csq(qr, qi);
          cr = __uint_as_float(sw[7][0]); ci = __uint_as_float(sw[7][1]);
#pragma unroll
          for (int u = 6; u >= 0; --u) {
            u0 = o_fnma(ni, ci, __uint_as_float(sw[u][0])); u1 = o_fma(ni, cr, __uint_as_float(sw[u][1]));
            cr = o_fma(nr, cr, u0); ci = o_fma(nr, ci, u1);
          }
          asm volatile("s_nop 1" : "+v"(cr), "+v"(ci));
          auto pa = __builtin_amdgcn_permlane32_swap(__float_as_uint(cr), __float_as_uint(cr), false, false);
          auto pb = __builtin_amdgcn_permlane32_swap(__float_as_uint(ci), __float_as_uint(ci), false, false);
          cr = __uint_as_float(pa[1]); ci = __uint_as_float(pb[1]);
          cmad(cr, ci, qr, qi, __uint_as_float(pa[0]), __uint_as_float(pb[0]));
#pragma unroll
          for (int u = 0; u < 7; ++u) {
            u0 = o_fnma(par[1].mi, ci, s2[u][0]); u1 = o_fma(par[1].mi, cr, s2[u][1]);
            u0 = o_fma(par[1].mr, cr, u0); u1 = o_fma(par[1].mr, ci, u1);
            if (u < 2 * (int)w + h) { cr = u0; ci = u1; }
          }
#pragma unroll
          for (int j = 0; j < 16; ++j) {
            u0 = o_fnma(par[1].li, ci, zr[1][j]); u1 = o_fma(par[1].li, cr, zi[1][j]);
            cr = o_fma(par[1].lr, cr, u0); ci = o_fma(par[1].lr, ci, u1);
            u0 = o_mul(par[1].ci, ci); u1 = o_mul(par[1].ci, cr);
            u0 = o_fms(par[1].cr, cr, u0); u1 = o_fma(par[1].cr, ci, u1);
            *(LAS unsigned*)(size_t)(twa[((j & 3) >> 1) + 2 * ((j >> 2) & 1)] + ((j & 3) + 8 * (j >> 2)) * 128) = o_cvt(u0, u1);
          }
#pragma unroll
          for (int q = 0; q < 4; ++q)
            st128(__builtin_bit_cast(u32x4, ldsr(tr_ad(q >> 1, q & 1))), rht, vo, mtbase + (unsigned)(56 + 2 * blk1 + (q & 1)) * 4096u + (q >> 1) * 1024);
        }
        SB();
      }
      if constexpr (Q == 120) { if (two) { wait_bar<16>(); st64(tag, rfl, vlead + FD_ + (rb * 16 + cg) * 8); } }
      if constexpr (Q == 180) fz = __builtin_amdgcn_raw_buffer_load_b64(rfl, FD_ + (rb * 16 + (lane & 15)) * 8, 0, SC1);
      if constexpr (Q == 32) fz = __builtin_amdgcn_raw_buffer_load_b64(rfl, FC_ + (rb * 16 + (lane & 15)) * 8, 0, SC1);
      if constexpr (Q == 96) fz = __builtin_amdgcn_raw_buffer_load_b64(rfl, FB_ + (rb * 16 + (lane & 15)) * 8, 0, SC1);
      if constexpr (go && (n1 == 72 || n1 == 126 || n1 == 198)) {
        const unsigned o = (n1 == 72 ? FC_ : (n1 == 126 ? FB_ : FD_)) + (rb * 16 + (lane & 15)) * 8;
        const bool need = n1 == 126 || (n1 == 72 ? (lane & 15) < 11 : ((lane & 15) >= 11 && (lane & 15) < 15));
        if (!flag_ok(fz, need)) spin(o, need);
        asm volatile("" ::: "memory");
        SB();
      }
#pragma unroll
      for (int q = 2 * g; q < 2 * g + 2; ++q) {
        const int m = q / 5, n = q % 5;
        const f32x4 z = {0.f, 0.f, 0.f, 0.f};
        if (s >= 14) accS[m][n] = MFMA16(fb[s % DEP][n], fa[s % DEP][m], s == 14 ? z : accS[m][n]);
        else acc[m][n] = MFMA16(fb[s % DEP][n], fa[s % DEP][m], s == 0 ? z : acc[m][n]);
      }
      if constexpr (go) ld(n1 / 9, n1 % 9);
      if constexpr (Q >= 120 && Q < 125) {
        constexpr int n = Q - 120;
        const unsigned i = 64 * n + lane, row = i / 20, ch = i - 20 * row;
        orow[n] = row | (ch << 8); PINV(orow[n]);
      }
      if constexpr (Q >= 130 && Q < 133) {
        constexpr int n = Q - 130;
        const unsigned o = 1024 * n + 16 * lane, row = o / 160;
        setm0<8192 + 1024 * n>(tb);
        dma<0>(row < 16 ? (unsigned)tau(16 * w + row) * 1280u + (o - row * 160) : OOB, c.rx, (unsigned)(64 * mt) * 1280u + 160u * nt);
      }
      if constexpr (Q >= 140 && Q < 190 && Q % 10 == 5) dv[(Q - 140) / 10] = *(const u32x2*)(p.dsk + 80 * nt + 4 * (orow[(Q - 140) / 10] >> 8));
      if constexpr (Q >= 140 && Q < 190 && Q % 10 == 0)
        gm[(Q - 140) / 10] = __uint_as_float(__builtin_amdgcn_raw_buffer_load_b32(mkrs(p.ws + G_OFF, G_BYTES), (64 * mt + 16 * w + (orow[(Q - 140) / 10] & 255)) * 4, 0, SC1));
      if constexpr (Q >= 190 && Q < 210 && Q % 5 == 0)
        gv[(Q - 190) / 5] = __uint_as_float(__builtin_amdgcn_raw_buffer_load_b32(mkrs(p.ws + G_OFF, G_BYTES), (64 * mt + 16 * ((Q - 190) / 5) + (lane & 15)) * 4, 0, SC1));
      if constexpr (Q < 7) {
        if constexpr (Q == 0) { cr = __uint_as_float(sv[7][0]); ci = __uint_as_float(sv[7][1]); }
        sg[Q] = *(const LAS f32x2*)(size_t)(segr + 256 * Q);
        cmad(cr, ci, nr, ni, __uint_as_float(sv[6 - Q][0]), __uint_as_float(sv[6 - Q][1])); PINV(cr); PINV(ci);
      }
      if constexpr (Q == 7) {
        auto a = __builtin_amdgcn_permlane32_swap(__float_as_uint(cr), __float_as_uint(cr), false, false);
        auto b = __builtin_amdgcn_permlane32_swap(__float_as_uint(ci), __float_as_uint(ci), false, false);
        cr = __uint_as_float(a[1]); ci = __uint_as_float(b[1]);
        cmad(cr, ci, qr, qi, __uint_as_float(a[0]), __uint_as_float(b[0])); PINV(cr); PINV(ci);
      }
      if constexpr (Q >= 8 && Q < 15) {
        float a = cr, b = ci;
        cmad(a, b, par[0].mr, par[0].mi, sg[Q - 8][0], sg[Q - 8][1]);
        if (Q - 8 < 2 * (int)w + h) { cr = a; ci = b; }
        PINV(cr); PINV(ci);
        if constexpr (Q < 12) { twa[Q - 8] = tw_ad(Q - 8); PINV(twa[Q - 8]); }
      }
      if constexpr (Q >= 15 && Q < 47) {
        constexpr int j = (Q - 15) >> 1;
        if constexpr (((Q - 15) & 1) == 0) {
          u0 = o_fnma(par[0].li, ci, zr[0][j]); u1 = o_fma(par[0].li, cr, zi[0][j]);
          cr = o_fma(par[0].lr, cr, u0); ci = o_fma(par[0].lr, ci, u1);
        } else {
          u0 = o_mul(par[0].ci, ci); u1 = o_mul(par[0].ci, cr);
          u0 = o_fms(par[0].cr, cr, u0); u1 = o_fma(par[0].cr, ci, u1);
          *(LAS unsigned*)(size_t)(twa[((j & 3) >> 1) + 2 * ((j >> 2) & 1)] + ((j & 3) + 8 * (j >> 2)) * 128) = o_cvt(u0, u1);
        }
      }
      if constexpr (Q == 47 || Q == 48) { tf[2 * (Q - 47)] = ldsr(tr_ad(Q - 47, 0)); tf[2 * (Q - 47) + 1] = ldsr(tr_ad(Q - 47, 1)); }
      if constexpr (Q >= 50 && Q < 54) st128(__builtin_bit_cast(u32x4, tf[Q - 50]), rht, vo, mtbase + (unsigned)(56 + 2 * cg + ((Q - 50) & 1)) * 4096u + ((Q - 50) >> 1) * 1024);
      SB();
    });
    STAMP(7);
    BST(88);
    LAS f32x4* red = (LAS f32x4*)smem;
    const int pw = ((lane + 4 * (lane >> 4)) & 15) + (lane & 48);
#pragma unroll
    for (int m = 0; m < 4; ++m) {
      const float g = gv[m], og = rbf(1.f - g);
#pragma unroll
      for (int n = 0; n < 5; ++n) red[(w * 20 + m * 5 + n) * 64 + pw] = accS[m][n] * g + acc[m][n] * og;
    }
    asm volatile("s_waitcnt vmcnt(0) lgkmcnt(0)\n\ts_barrier" ::: "memory");
    const RSRC ro = mkrs(p.out, L_ * D_ * 4);
#pragma unroll
    for (int n = 0; n < 5; ++n) {
      const unsigned row = orow[n] & 255, ch = orow[n] >> 8;
      const unsigned pr = (w * 5 + (ch >> 2)) * 64 + ((row + 4 * (ch & 3)) & 15) + 16 * (ch & 3);
      f32x4 s = red[pr];
#pragma unroll
      for (int u = 1; u < 4; ++u) s += red[u * 1280 + pr];
      const u32x2 xv = *(const LAS u32x2*)(size_t)(tb + 8192 + 512 * n + 8 * lane);
      s[0] += gm[n] * rbf(bf2f(xv[0] & 0xffffu) * bf2f(dv[n][0] & 0xffffu));
      s[1] += gm[n] * rbf(bf2f(xv[0] >> 16) * bf2f(dv[n][0] >> 16));
      s[2] += gm[n] * rbf(bf2f(xv[1] & 0xffffu) * bf2f(dv[n][1] & 0xffffu));
      s[3] += gm[n] * rbf(bf2f(xv[1] >> 16) * bf2f(dv[n][1] >> 16));
      st128(__builtin_bit_cast(u32x4, s), ro, (unsigned)(tau(16 * w + row) * 640 + 4 * ch) * 4u, (unsigned)(64 * mt * 640 + 80 * nt) * 4u);
    }
    if (bid == 0) {
      const u64 e1 = ep + 1;
      st64((u32x2){(unsigned)e1, (unsigned)(e1 >> 32)}, rfl, vlead + EP_);
    }
  }
  STAMP(8);
  asm volatile("fused_code_end:");
}

extern "C" void kernel_launch(void* const* d_in, const int* in_sizes, int n_in, void* d_out, int out_size, void* d_ws, size_t ws_size, hipStream_t stream) {
  Args a;
  a.x = (const u16*)d_in[0]; a.lre = (const u16*)d_in[1]; a.lim = (const u16*)d_in[2]; a.bre = (const u16*)d_in[3]; a.bim = (const u16*)d_in[4];
  a.cre = (const u16*)d_in[5]; a.cim = (const u16*)d_in[6]; a.ldt = (const u16*)d_in[7]; a.dsk = (const u16*)d_in[8];
  a.w1 = (const u16*)d_in[10]; a.w3 = (const u16*)d_in[11]; a.w2 = (const u16*)d_in[12]; a.gw = (const u16*)d_in[13];
  a.ws = (char*)d_ws; a.out = (float*)d_out;
  (void)hipFuncSetAttribute((const void*)fused, hipFuncAttributeMaxDynamicSharedMemorySize, LDS1);
  fused<<<256, 256, LDS1, stream>>>(a);
}
```

```cpp
#include <hip/hip_runtime.h>
#include <hip/hip_bf16.h>
#include <utility>

typedef unsigned short u16;
typedef short bf16x8 __attribute__((ext_vector_type(8)));
typedef short s16x4 __attribute__((ext_vector_type(4)));
typedef float f32x2 __attribute__((ext_vector_type(2)));
typedef float f32x4 __attribute__((ext_vector_type(4)));
typedef float f32x16 __attribute__((ext_vector_type(16)));
typedef unsigned u32x2 __attribute__((ext_vector_type(2)));
typedef unsigned u32x4 __attribute__((ext_vector_type(4)));
typedef __bf16 bf2_t __attribute__((ext_vector_type(2)));
typedef unsigned long long u64;
#define LAS __attribute__((address_space(3)))
#define DI __device__ __forceinline__
#define SB() __builtin_amdgcn_sched_barrier(0)
#define RSRC __amdgpu_buffer_rsrc_t
#define RHO 3
#define PRE_T 15
#define PRE_E 12
#define CPF 1
#define CPF_ALL 0
#define TLBW 1
#define QL 212
#define TOUCH 0
constexpr int SC1 = 16;
constexpr unsigned OOB = 0x7fffff00u;

constexpr int L_ = 2048, D_ = 640, P_ = 640, F_ = 1728;
constexpr int KS2 = 96;
constexpr size_t HT_OFF = 0, HT_BYTES = (size_t)32 * KS2 * 4096;
constexpr size_t BT_OFF = HT_OFF + HT_BYTES, BT_BYTES = (size_t)8 * KS2 * 5120;
constexpr size_t G_OFF = BT_OFF + BT_BYTES, G_BYTES = 2048 * 4;
constexpr size_t ST_OFF = G_OFF + G_BYTES, ST_BYTES = 16 * 640 * 8;
constexpr size_t FL_OFF = ST_OFF + ST_BYTES;
constexpr unsigned FS_ = 0, FA_ = 32768, FC_ = 65536, FB_ = 98304, FD_ = 131072, FG_ = 163840, EP_ = 165888, FL_BYTES = 166016;
constexpr size_t STAMP_OFF = FL_OFF + 262144;

constexpr unsigned RING = 0, XST = 81920, SCR = 147456, LDS1 = 163840;

template <int B> DI unsigned lt_mask(int a) { unsigned m; asm("s_sub_i32 %0, %1, %2\n\ts_ashr_i32 %0, %0, 31" : "=s"(m) : "s"(a), "n"(B) : "scc"); return m; }
DI unsigned ssel(unsigned m, unsigned a, unsigned b) { return b ^ ((a ^ b) & m); }
DI const void* psel(unsigned m, const void* a, const void* b) { return (const void*)((u64)b ^ (((u64)a ^ (u64)b) & (((u64)m << 32) | m))); }
DI unsigned cvtpk(float lo, float hi) { f32x2 v = {lo, hi}; bf2_t b = __builtin_convertvector(v, bf2_t); return __builtin_bit_cast(unsigned, b); }
DI float bf2f(unsigned v) { return __uint_as_float(v << 16); }
DI float rbf(float x) { return __uint_as_float(cvtpk(x, 0.f) << 16); }
DI s16x4 trr(unsigned a) { return __builtin_amdgcn_ds_read_tr16_b64_v4i16((LAS s16x4*)(size_t)a); }
DI bf16x8 ldsr(unsigned a) { return *(const LAS bf16x8*)(size_t)a; }
DI bf16x8 cat(s16x4 a, s16x4 b) { return __builtin_shufflevector(a, b, 0, 1, 2, 3, 4, 5, 6, 7); }
template <int IMM> DI void dma(unsigned voff, RSRC rs, unsigned soff) {
  asm volatile("buffer_load_dwordx4 %0, %1, %2 offen offset:%3 lds" ::"v"(voff), "s"(rs), "s"(soff), "n"(IMM) : "memory");
}
template <int IMM> DI void dma2(unsigned voff, RSRC rs, unsigned s0, unsigned s1) {
  unsigned t;
  asm volatile("s_add_u32 %0, %3, %4\n\tbuffer_load_dwordx4 %1, %2, %0 offen offset:%5 lds" : "=&s"(t) : "v"(voff), "s"(rs), "s"(s0), "s"(s1), "n"(IMM) : "memory", "scc");
}
template <int IMM, int ADD> DI void dma2i(unsigned voff, RSRC rs, unsigned s0) {
  unsigned t;
  asm volatile("s_add_u32 %0, %3, %4\n\tbuffer_load_dwordx4 %1, %2, %0 offen offset:%5 lds" : "=&s"(t) : "v"(voff), "s"(rs), "s"(s0), "n"(ADD), "n"(IMM) : "memory", "scc");
}
template <int IMM> DI void setm0(unsigned v) { asm volatile("s_add_u32 m0, %0, %1\n\ts_nop 0" ::"s"(v), "n"(IMM) : "memory", "scc"); }
#define PINS(x) asm volatile("" : "+s"(x))
template <int N> DI void wait_bar() { asm volatile("s_waitcnt vmcnt(%0)\n\ts_barrier" ::"n"(N) : "memory"); }
#define BST(slot)
template <int N> DI void wait_vm() { asm volatile("s_waitcnt vmcnt(%0)" ::"n"(N) : "memory"); }
DI RSRC mkrs(const void* p, unsigned bytes) { return __builtin_amdgcn_make_buffer_rsrc((void*)p, 0, bytes, 0x00020000); }
DI void st128(u32x4 v, RSRC r, unsigned vo, unsigned so) { __builtin_amdgcn_raw_buffer_store_b128(v, r, vo, so, SC1); asm volatile("s_nop 1" ::"v"(v) : "memory"); }
DI void st64(u32x2 v, RSRC r, unsigned vo) { __builtin_amdgcn_raw_buffer_store_b64(v, r, vo, 0, SC1); }
DI u32x2 ld64_async(RSRC r, unsigned vo) { u32x2 v; asm volatile("buffer_load_dwordx2 %0, %1, %2, 0 offen sc1" : "=v"(v) : "v"(vo), "s"(r) : "memory"); return v; }
DI u32x2 ld64_sync(RSRC r, unsigned vo) { u32x2 v; asm volatile("buffer_load_dwordx2 %0, %1, %2, 0 offen sc1\n\ts_waitcnt vmcnt(0)" : "=&v"(v) : "v"(vo), "s"(r) : "memory"); return v; }
#define PINV(x) asm volatile("" : "+v"(x))
#define PINA(x) asm volatile("" : "+a"(x))
#define MFMA16(a, b, c) __builtin_amdgcn_mfma_f32_16x16x32_bf16((a), (b), (c), 0, 0, 0)
DI void mf_xa0(f32x16& d, const bf16x8& x, const bf16x8& w) { asm volatile("v_mfma_f32_32x32x16_bf16 %0, %1, %2, 0" : "=&v"(d) : "a"(x), "v"(w)); }
DI void mf_xa(f32x16& d, const bf16x8& x, const bf16x8& w) { asm volatile("v_mfma_f32_32x32x16_bf16 %0, %1, %2, %0" : "+v"(d) : "a"(x), "v"(w)); }
DI void mf_xb0(f32x16& d, const bf16x8& w, const bf16x8& x) { asm volatile("v_mfma_f32_32x32x16_bf16 %0, %1, %2, 0" : "=&v"(d) : "v"(w), "a"(x)); }
DI void mf_xb(f32x16& d, const bf16x8& w, const bf16x8& x) { asm volatile("v_mfma_f32_32x32x16_bf16 %0, %1, %2, %0" : "+v"(d) : "v"(w), "a"(x)); }
DI void mf16_0(f32x4& d, const bf16x8& a, const bf16x8& b) { asm volatile("v_mfma_f32_16x16x32_bf16 %0, %1, %2, 0" : "=&v"(d) : "a"(a), "v"(b)); }
DI void mf16(f32x4& d, const bf16x8& a, const bf16x8& b) { asm volatile("v_mfma_f32_16x16x32_bf16 %0, %1, %2, %0" : "+v"(d) : "a"(a), "v"(b)); }
DI void mfma_done(f32x16& a, f32x16& b) { asm volatile("s_nop 7\n\ts_nop 7" : "+v"(a), "+v"(b)); }
DI void mfma_done4(f32x16 (&a)[4]) { asm volatile("s_nop 7\n\ts_nop 7" : "+v"(a[0]), "+v"(a[1]), "+v"(a[2]), "+v"(a[3])); }
#define STAMP(i) do {} while (0)

template <int V> using IC = std::integral_constant<int, V>;
template <class Fn, int... Is> DI void sfor_(Fn&& f, std::integer_sequence<int, Is...>) { (f(IC<Is>{}), ...); }
template <int N, class Fn> DI void sfor(Fn&& f) { sfor_(f, std::make_integer_sequence<int, N>{}); }

struct Args {
  const u16 *x, *lre, *lim, *bre, *bim, *cre, *cim, *ldt, *dsk, *w1, *w3, *w2, *gw;
  char* ws;
  float* out;
};

struct Stream {
  RSRC ra, rb;
  unsigned v0, v1, v2, v3;
  unsigned k, end, adv, lim, d1, d2, d3;
};
template <int Q> DI void st_piece(const Stream& s) {
  if constexpr (Q == 0) dma<0>(s.v0, s.ra, s.k);
  else if constexpr (Q == 1) dma2<1024>(s.v1, s.ra, s.k, s.d1);
  else if constexpr (Q == 2) dma2<2048>(s.v2, s.rb, s.k, s.d2);
  else dma2<3072>(s.v3, s.rb, s.k, s.d3);
}
DI void st_adv(Stream& s) { PINS(s.k); const unsigned k = s.k + s.adv; s.k = (k >= s.end) ? k - s.lim : k; }

struct Ctx {
  RSRC rx, rB, rw1, rw3, rnull;
  unsigned vS0, vS1, vS2, vS3, vD;
  unsigned lds0, m0ring;
  unsigned rho, w;
};
DI void stream_S(Stream& s, const Ctx& c, int i) {
  s.ra = c.rB; s.rb = c.rB; s.v0 = c.vS0; s.v1 = c.vS1; s.v2 = c.vS2; s.v3 = c.vS3;
  const unsigned base = (unsigned)(32 * i + 16 * (c.w & 1)) * 1280u;
  s.k = base + c.rho * 256u; s.end = base + 1280u; s.adv = 256u; s.lim = 1280u;
  s.d1 = 4096u; s.d2 = 8192u; s.d3 = 12288u;
}
DI void stream_D(Stream& s, const Ctx& c, int j) {
  s.ra = c.rw1; s.rb = c.rw3; s.v0 = s.v1 = s.v2 = s.v3 = c.vD;
  s.k = (unsigned)j * 128u + c.rho * (128u * 3456u); s.end = 640u * 3456u; s.adv = 64u * 3456u; s.lim = 640u * 3456u;
  s.d1 = 8u * 3456u - 1024u; s.d2 = 0u; s.d3 = 8u * 3456u - 1024u;
}
DI void stream_null(Stream& s, const Ctx& c) {
  s.ra = c.rnull; s.rb = c.rnull; s.k = 0; s.end = 1; s.adv = 0; s.lim = 0; s.d1 = s.d2 = s.d3 = 0;
}

struct Par { float lr, li, cr, ci, mr, mi; };
DI void csq(float& a, float& b) { const float r = a * a - b * b, i = 2.f * a * b; a = r; b = i; }
DI Par ssm_par(unsigned ulre, unsigned ulim, unsigned uldt) {
  const float Lr = bf2f(ulre), Li = bf2f(ulim);
  const float dt = rbf(expf(bf2f(uldt)));
  const float e = expf(dt * Lr), th = dt * Li;
  const float n = __builtin_rintf(th * 0.636619772f);
  float y = fmaf(n, -1.57079601287841796875f, th);
  y = fmaf(n, -3.1391647326017846353352069854736328125e-7f, y);
  y = fmaf(n, -5.390302529957764765544681040410068817436695098876953125e-15f, y);
  const float y2 = y * y;
  float ps = fmaf(y2, 2.6083159809786593541503e-06f, -0.0001981069071916863322258f);
  ps = fmaf(ps, y2, 0.00833307858556509017944336f); ps = fmaf(ps, y2, -0.166666597127914428710938f);
  const float sn = fmaf(ps * y2, y, y);
  float pc = fmaf(y2, -2.71811842367242206819355e-07f, 2.47990446951007470488548e-05f);
  pc = fmaf(pc, y2, -0.00138888787478208541870117f); pc = fmaf(pc, y2, 0.0416666641831398010253906f); pc = fmaf(pc, y2, -0.5f);
  const float cs = fmaf(pc, y2, 1.f);
  const int q = (int)n;
  float sv = (q & 1) ? cs : sn, cv = (q & 1) ? sn : cs;
  sv = (q & 2) ? -sv : sv; cv = ((q + 1) & 2) ? -cv : cv;
  Par r; r.lr = e * cv; r.li = e * sv;
  const float nr = r.lr - 1.f, ni = r.li, den = Lr * Lr + Li * Li;
  r.cr = 2.f * ((nr * Lr + ni * Li) / den); r.ci = 2.f * ((ni * Lr - nr * Li) / den);
  r.mr = r.lr; r.mi = r.li;
  csq(r.mr, r.mi); csq(r.mr, r.mi); csq(r.mr, r.mi); csq(r.mr, r.mi);
  return r;
}
DI void pin_par(Par& p) { asm volatile("" : "+v"(p.lr), "+v"(p.li), "+v"(p.cr), "+v"(p.ci), "+v"(p.mr), "+v"(p.mi)); }
DI unsigned ldu16(const u16* p, unsigned vo) { unsigned r; asm volatile("global_load_ushort %0, %1, %2" : "=v"(r) : "v"(vo), "s"(p) : "memory"); return r; }
DI float o_mul(float a, float b) { float r; asm volatile("v_mul_f32 %0, %1, %2" : "=v"(r) : "v"(a), "v"(b)); return r; }
DI float o_mulk(float a) { float r; asm volatile("v_mul_f32 %0, 0xbfb8aa3b, %1" : "=v"(r) : "v"(a)); return r; }
DI float o_add1(float a) { float r; asm volatile("v_add_f32 %0, 1.0, %1" : "=v"(r) : "v"(a)); return r; }
DI float o_exp(float a) { float r; asm volatile("v_exp_f32 %0, %1" : "=v"(r) : "v"(a)); return r; }
DI float o_rcp(float a) { float r; asm volatile("v_rcp_f32 %0, %1" : "=v"(r) : "v"(a)); return r; }
DI float o_fma(float a, float b, float c) { float r; asm volatile("v_fma_f32 %0, %1, %2, %3" : "=v"(r) : "v"(a), "v"(b), "v"(c)); return r; }
DI float o_fnma(float a, float b, float c) { float r; asm volatile("v_fma_f32 %0, -%1, %2, %3" : "=v"(r) : "v"(a), "v"(b), "v"(c)); return r; }
DI float o_fms(float a, float b, float c) { float r; asm volatile("v_fma_f32 %0, %1, %2, -%3" : "=v"(r) : "v"(a), "v"(b), "v"(c)); return r; }
DI float o_add(float a, float b) { float r; asm volatile("v_add_f32 %0, %1, %2" : "=v"(r) : "v"(a), "v"(b)); return r; }
DI unsigned o_cvt(float a, float b) { unsigned r; asm volatile("v_cvt_pk_bf16_f32 %0, %1, %2" : "=v"(r) : "v"(a), "v"(b)); return r; }
DI void cmad(float& a, float& b, float mr, float mi, float u, float v) {
  const float x = fmaf(mr, a, fmaf(-mi, b, u)), y = fmaf(mr, b, fmaf(mi, a, v));
  a = x; b = y;
}

template <class Next, class Bar, class Gap>
DI void s_body(const bf16x8 (&xf)[40], f32x16& zre, f32x16& zim, Stream& st, const Ctx& c, const unsigned (&sa)[8], Next next, Bar bar, Gap gap) {
  bf16x8 wr[3], wi[3];
  auto rd = [&](int ks, int im) -> bf16x8 {
    const int n = ks >> 3, s = ks & 7;
    return n < 4 ? ldsr(sa[s] + n * 16384 + im * 8192) : ldsr(sa[s] + 65536u + im * 8192);
  };
  BST(20); wait_bar<8>(); BST(21);
  setm0<3 * 16384>(c.m0ring);
  wr[0] = rd(0, 0); wi[0] = rd(0, 1); wr[1] = rd(1, 0); wi[1] = rd(1, 1);
  st_piece<0>(st);
  SB();
  sfor<80>([&](auto GG) {
    constexpr int G = GG, ks = G >> 1, im = G & 1;
    if constexpr (im == 0 && (ks & 7) == 6 && ks < 38) {
      constexpr int n = (ks >> 3) + 1;
      BST(20 + 2 * n); wait_bar<8>(); BST(21 + 2 * n);
      bar(IC<n>{});
      if constexpr (n == 2) next(st);
      setm0<((n + 3) % 5) * 16384>(c.m0ring);
      SB();
    }
    if constexpr (ks == 0) { if constexpr (im) mf_xa0(zim, xf[0], wi[0]); else mf_xa0(zre, xf[0], wr[0]); }
    else { if constexpr (im) mf_xa(zim, xf[ks], wi[ks % 3]); else mf_xa(zre, xf[ks], wr[ks % 3]); }
    SB();
    if constexpr (im == 0 && (ks & 1) == 0 && ks <= 36) {
      constexpr int q = ((ks + 2) >> 1) & 3;
      st_piece<q>(st);
      if constexpr (q == 3) st_adv(st);
    }
    if constexpr (ks + 2 < 40) { if constexpr (im) wi[(ks + 2) % 3] = rd(ks + 2, 1); else wr[(ks + 2) % 3] = rd(ks + 2, 0); }
    gap(GG);
    SB();
  });
  mfma_done(zre, zim);
}

template <bool FIRST, int W8, int W9, class Next, class Bar, class Gap, class Pre>
DI void d_body(bf16x8 (&xf)[40], f32x16 (&acc)[4], Stream& st, const Ctx& c, const unsigned (&rbd)[4], const unsigned (&sa)[8], unsigned xso,
               Next next, Bar bar, Gap gap, Pre pre) {
  s16x4 wl[2][4], wh[2][4];
  auto rd1 = [&](int ks, int t, int half) -> s16x4 {
    const unsigned off = ((ks >> 2) % 5) * 16384 + (ks & 3) * 4096 + (t >> 1) * 2048 + half * 512;
    return trr(off >= 49152 ? rbd[2 + (t & 1)] + (off - 49152) : rbd[t & 1] + off);
  };
  const unsigned xdelta = XST + c.w * 16384u;
  unsigned xk = xso + c.rho * 256u;
  const unsigned xm0 = c.lds0 + xdelta;
  auto xpiece = [&](auto M, auto Q) {
    constexpr int m = M, q = Q;
    if constexpr (q == 0 && m > 0) { PINS(xk); xk += 256u; xk = xk >= xso + 1280u ? xk - 1280u : xk; }
    if constexpr ((q & 3) == 0) setm0<(m & 1) * 8192 + (q >> 2) * 4096>(xm0);
    constexpr int ro = (16 * (q & 1) + 4 * (q >> 1)) * 1280 - 1024 * (q & 3);
    dma2i<1024 * (q & 3), ro>((q & 3) == 0 ? c.vS0 : ((q & 3) == 1 ? c.vS1 : ((q & 3) == 2 ? c.vS2 : c.vS3)), c.rx, xk);
  };
  auto wpiece = [&](auto N, auto Q) {
    constexpr int n = N, q = Q;
    if constexpr (q == 0) { if constexpr (n == 10) next(st); setm0<(n % 5) * 16384>(c.m0ring); }
    st_piece<q>(st);
    if constexpr (q == 3) st_adv(st);
  };
  if constexpr (FIRST) {
    sfor<8>([&](auto Q) { xpiece(IC<0>{}, Q); });
    sfor<4>([&](auto Q) { wpiece(IC<0>{}, Q); });
    sfor<4>([&](auto Q) { wpiece(IC<1>{}, Q); });
    sfor<4>([&](auto Q) { xpiece(IC<1>{}, Q); });
    SB();
    pre(IC<-1>{});
    SB();
  }
  BST(FIRST ? 0 : 40); wait_bar<8>(); BST(FIRST ? 1 : 41);
  bar(IC<0>{});
  if constexpr (FIRST) {
#pragma unroll
    for (int s = 0; s < 8; ++s) xf[s] = ldsr(sa[s] + xdelta);
    SB();
  } else setm0<3 * 16384>(c.m0ring);
#pragma unroll
  for (int t = 0; t < 4; ++t) { wl[0][t] = rd1(0, t, 0); wh[0][t] = rd1(0, t, 1); }
  if constexpr (!FIRST) st_piece<0>(st);
  SB();
  sfor<160>([&](auto GG) {
    constexpr int G = GG, ks = G >> 2, t = G & 3;
    if constexpr (t == 0 && (ks & 3) == 3 && ks < 39) {
      constexpr int n = (ks >> 2) + 1;
      BST((FIRST ? 0 : 40) + 2 * n);
      wait_bar<FIRST ? ((n & 1) ? 10 : 6) : (n == 8 ? W8 : (n == 9 ? W9 : 8))>();
      BST((FIRST ? 1 : 41) + 2 * n);
      bar(IC<n>{});
      if constexpr (!FIRST && n < 7) setm0<((n + 3) % 5) * 16384>(c.m0ring);
      SB();
    }
    const bf16x8 wf = cat(wl[ks & 1][t], wh[ks & 1][t]);
    if constexpr (ks == 0) mf_xb0(acc[t], wf, xf[0]); else mf_xb(acc[t], wf, xf[ks]);
    SB();
    if constexpr (FIRST) {
      if constexpr ((t & 1) == 0) {
        constexpr int s = G >> 1;
        if constexpr (s < 4) xpiece(IC<1>{}, IC<4 + s>{});
        else if constexpr (s < 60) {
          constexpr int u = (s - 4) % 16, m = (s - 4) / 16;
          if constexpr (u < 8) wpiece(IC<2 + 2 * m + (u >> 2)>{}, IC<u & 3>{}); else xpiece(IC<2 + m>{}, IC<u - 8>{});
        } else if constexpr (s < 68) wpiece(IC<10 + ((s - 60) >> 2)>{}, IC<s & 3>{});
        else if constexpr (s < 70) pre(IC<s - 68>{});
        else if constexpr (s < 74) wpiece(IC<12>{}, IC<s - 70>{});
        else if constexpr (s < 79) pre(IC<s - 72>{});
      }
      constexpr int g8 = G - 28;
      if constexpr (g8 >= 0 && (g8 & 31) < 8 && (g8 >> 5) < 4) xf[8 * ((g8 >> 5) + 1) + (g8 & 7)] = ldsr(sa[g8 & 7] + xdelta + (((g8 >> 5) + 1) & 1) * 8192);
    } else if constexpr (t == 0 && ks < 27) {
      st_piece<(ks + 1) & 3>(st);
      if constexpr (((ks + 1) & 3) == 3) st_adv(st);
    }
    if constexpr (ks < 39) { wl[(ks + 1) & 1][t] = rd1(ks + 1, t, 0); wh[(ks + 1) & 1][t] = rd1(ks + 1, t, 1); }
    gap(GG);
    SB();
  });
  mfma_done4(acc);
}

__global__ __launch_bounds__(256, 1) void fused(Args p) {
  extern __shared__ __attribute__((aligned(16))) char smem[];
  const int lane = threadIdx.x & 63;
  const unsigned w = __builtin_amdgcn_readfirstlane(threadIdx.x >> 6);
  const int bid = blockIdx.x, xcd = bid & 7, r = bid >> 3, idx = r >> 2;
  const int rb = (xcd >> 1) * 4 + (r & 3), cg = 2 * idx + (xcd & 1);
  STAMP(0);
  Ctx c;
  c.w = w; c.rho =
    (unsigned)(rb + idx) % 3u * 2u;
  c.lds0 = (unsigned)(size_t)smem; c.m0ring = c.lds0 + RING + w * 4096u;
  BST(62);
  const int l32 = lane & 31, h = lane >> 5;
  const bool heavy = cg < 11, two = cg >= 11 && cg < 15;
  const unsigned mH = lt_mask<11>(cg), m2 = lt_mask<15>(cg) & ~mH, mW = lt_mask<2>(w);
  const int blk1 = cg + (5 & m2);
  unsigned cpf;
  {
    u64 pc; unsigned len;
    asm volatile("s_getpc_b64 %0\n1:\n\ts_mov_b32 %1, fused_code_end-1b\n\ts_max_u32 %1, %1, fused_side_end-1b" : "=s"(pc), "=s"(len)::"scc");
    asm volatile("buffer_load_dword %0, %1, %2, 0 offen" : "=v"(cpf) : "v"((r * 20 + w * 5 + lane) * 128u | (lane < 5 ? 0u : OOB)), "s"(mkrs((const void*)pc, CPF ? len : 0)) : "memory");
  }
  unsigned raw[2][3];
#pragma unroll
  for (int i = 0; i < 2; ++i) {
    const unsigned ch = (32 * (i ? blk1 : cg) + l32) * 2;
    raw[i][0] = ldu16(p.lre, ch); raw[i][1] = ldu16(p.lim, ch); raw[i][2] = ldu16(p.ldt, ch);
  }
  const unsigned* tlp;
  asm volatile("global_load_dwordx2 %0, %1, %2" : "=v"(tlp) : "v"((lane < 15 ? lane : 0) * 8), "s"(__builtin_amdgcn_kernarg_segment_ptr()) : "memory");
  c.rx = mkrs(p.x, L_ * D_ * 2); c.rB = mkrs(psel(mW, p.bre, p.bim), P_ * D_ * 2);
  c.rw1 = mkrs(p.w1, D_ * F_ * 2); c.rw3 = mkrs((const char*)p.w3 - 2048, D_ * F_ * 2 + 2048); c.rnull = mkrs(p.x, 0);
  {
    const unsigned rr = lane >> 4, t = (lane & 15) ^ rr;
    c.vS0 = rr * 1280 + t * 16; c.vS1 = rr * 1280 + (t ^ 4) * 16; c.vS2 = rr * 1280 + (t ^ 8) * 16; c.vS3 = rr * 1280 + (t ^ 12) * 16;
    const unsigned lrow = lane >> 3, lch = (lane & 7) ^ (((lrow >> 1) & 1) << 2);
    c.vD = (lrow + 16 * w) * 3456 + lch * 16;
  }
  u64 ep = *(const __attribute__((address_space(4))) u64*)(size_t)(p.ws + FL_OFF + EP_);
  bf16x8 xf[40];
  Par par[2];
  Stream st;
  stream_D(st, c, cg);
  unsigned sa[8], rbd[4];
  const unsigned tb = c.lds0 + XST + w * 16384;
  int v = ((rb * 16 + cg) >> 3) * 4 + w, ft, ntw, ci_, ct;
  unsigned hasw, hasc;

  Stream st2;
  RSRC rbw, rbc;
  unsigned sow, soc;
  f32x16 accp[4];
  {
    unsigned pv[6];
    d_body<true, 0, 0>(xf, accp, st, c, rbd, sa, (unsigned)(128 * rb + 32 * w) * 1280u,
      [&](Stream& s) { PINS(c.rho); stream_S(s, c, cg); },
      [&](auto) {},
      [&](auto GG) {
        constexpr int G = GG;
        if constexpr (G == 119) {
          PINV(cpf);
          PINS(v);
          ft = v >> 1; ntw = cg & 7; ci_ = v / 5; ct = 5 * ntw + v - 5 * ci_;
          hasw = lt_mask<54>(ft) & ((v & 1) - 1u); hasc = lt_mask<100>(v);
        }
        if constexpr (G >= 121 && G < 131 && (G & 1)) {
          constexpr int i = (G - 121) >> 1;
          const unsigned o = 1024 * i + 16 * lane, row = o / 160;
          pv[i] = row * 1120 + o; PINV(pv[i]);
        }
        if constexpr (G == 133) {
          rbw = mkrs(p.ws + BT_OFF, ssel(hasw, BT_BYTES, 0)); rbc = mkrs(p.ws + BT_OFF, ssel(hasc, BT_BYTES, 0));
          sow = (unsigned)((ntw * KS2 + ft) * 5) * 1024u; PINS(sow);
        }
        if constexpr (G == 135) { soc = (unsigned)((ntw * KS2 + 56 + 2 * ci_) * 5 + ct - 5 * ntw) * 1024u; PINS(soc); }
        if constexpr (G == 101) {
          PINS(c.rho);
          const unsigned base = (unsigned)(32 * (cg + 5) + 16 * (c.w & 1)) * 1280u;
          st2.k = ssel(mH, (unsigned)(16 + cg) * 128u + c.rho * (128u * 3456u), base + c.rho * 256u);
          st2.end = ssel(mH, 640u * 3456u, base + 1280u);
          PINS(st2.k); PINS(st2.end);
        }
        if constexpr (G == 103) {
          st2.adv = ssel(mH, 64u * 3456u, 256u); st2.lim = ssel(mH, 640u * 3456u, 1280u); st2.d1 = ssel(mH, 8u * 3456u - 1024u, 4096u);
          PINS(st2.adv); PINS(st2.lim); PINS(st2.d1);
        }
        if constexpr (G == 105) { st2.d2 = ssel(mH, 0u, 8192u); st2.d3 = ssel(mH, 8u * 3456u - 1024u, 12288u); PINS(st2.d2); PINS(st2.d3); }
        if constexpr (G == 111) {
          const void* pb = psel(mW, p.bre, p.bim);
          const unsigned nb = ssel(m2, P_ * D_ * 2, 0);
          st2.ra = mkrs(psel(mH, p.w1, pb), ssel(mH, D_ * F_ * 2, nb));
          st2.rb = mkrs(psel(mH, (const char*)p.w3 - 2048, pb), ssel(mH, D_ * F_ * 2 + 2048, nb));
          PINS(st2.ra); PINS(st2.rb);
        }
        if constexpr (G == 107) {
          const unsigned hm = mH;
          st2.v0 = (c.vD & hm) | (c.vS0 & ~hm); st2.v1 = (c.vD & hm) | (c.vS1 & ~hm); PINV(st2.v0); PINV(st2.v1);
        }
        if constexpr (G == 109) {
          const unsigned hm = mH;
          st2.v2 = (c.vD & hm) | (c.vS2 & ~hm); st2.v3 = (c.vD & hm) | (c.vS3 & ~hm); PINV(st2.v2); PINV(st2.v3);
        }
        if constexpr (G == 131) { pv[5] = (unsigned)(16 * ct + (lane >> 2)) * 1280u + 64u * ci_ + 16u * (lane & 3); PINV(pv[5]); }
      },
      [&](auto I) {
        constexpr int i = I;
        if constexpr (i < 0) {
          int ln = lane; PINV(ln);
          const unsigned m32 = ln & 31, hh = ln >> 5;
#pragma unroll
          for (int s = 0; s < 8; ++s) { sa[s] = c.lds0 + m32 * 256 + (((2 * s + hh) ^ (m32 & 15)) << 4); PINV(sa[s]); }
          const unsigned blk = (ln >> 4) & 1, q = (ln & 15) >> 2, pp = ln & 3;
          const unsigned row = 8 * hh + q, ch = 2 * blk + (pp >> 1);
          const unsigned a = c.lds0 + row * 128 + ((ch ^ (((row >> 1) & 1) << 2)) * 16) + 8 * (pp & 1);
          rbd[0] = a; rbd[1] = a ^ 64; rbd[2] = a + 49152; rbd[3] = (a ^ 64) + 49152;
          asm volatile("s_waitcnt vmcnt(20)" : "+v"(raw[0][0]), "+v"(raw[0][1]), "+v"(raw[0][2]), "+v"(raw[1][0]), "+v"(raw[1][1]), "+v"(raw[1][2]), "+s"(ep), "+v"(cpf), "+v"(tlp),
                       "+v"(rbd[0]), "+v"(rbd[1]), "+v"(rbd[2]), "+v"(rbd[3])::"memory");
          if (TLBW) asm volatile("global_load_dword %0, %1, off" : "=v"(cpf) : "v"(tlp) : "memory");
          par[0] = ssm_par(raw[0][0], raw[0][1], raw[0][2]); pin_par(par[0]);
        } else {
          setm0<8192 + 1024 * i>(tb);
          if constexpr (i < 5) dma<0>(pv[i], mkrs(p.w2, ssel(hasw, F_ * D_ * 2, 0)), (unsigned)(32 * ft) * 1280u + 160u * ntw);
          else dma<0>(pv[5], mkrs(i == 5 ? p.cre : p.cim, ssel(hasc, D_ * P_ * 2, 0)), 0);
        }
      });
  }
  STAMP(1);
  const u64 tag64 = ep * 0x9E3779B97F4A7C15ull + 0x1234567ull;
  const u32x2 tag = {(unsigned)tag64, (unsigned)(tag64 >> 32)};
  const RSRC rfl = mkrs(p.ws + FL_OFF, FL_BYTES);
  const RSRC rst = mkrs(p.ws + ST_OFF, ST_BYTES);
  const RSRC rht = mkrs(p.ws + HT_OFF, HT_BYTES);
  const unsigned mtbase = (unsigned)((2 * rb + (w >> 1)) * KS2 * 4 + 2 * (w & 1)) * 1024u;
  const unsigned vlead = threadIdx.x == 0 ? 0u : OOB;
  const unsigned segw = c.lds0 + SCR + ((2 * w + h) * 32 + l32) * 8, segr = c.lds0 + SCR + l32 * 8;
  const unsigned vst = (unsigned)((l32 >> 4) * 1024 + h * 256 + (l32 & 15) * 16);
  const unsigned vo = lane * 16;

  f32x16 zr[2], zi[2];
  {
    float ta[3], tp[2], hv[2];
    unsigned pk[4];
    s16x4 t0[2], t1[2];
    u32x4 ca, cb2;
    const unsigned sof = mtbase + (unsigned)(2 * cg) * 4096u;
    s_body(xf, zr[0], zi[0], st, c, sa,
      [&](Stream& s) { s = st2; },
      [&](auto) {},
      [&](auto GG) {
        constexpr int G = GG;
        constexpr int P = G / 5, ph = G % 5, a = 2 * P, b = a + 1;
        auto ga = [&](int e) { return accp[e >> 4][e & 15]; };
        auto up = [&](int e) { return accp[2 + (e >> 4)][e & 15]; };
        if constexpr (ph == 0) {
          ta[0] = o_mulk(ga(a)); ta[1] = o_mulk(ga(b));
          if constexpr (P > 0) { hv[1] = o_mul(tp[1], ta[2]); }
        }
        if constexpr (ph == 1) {
          ta[0] = o_exp(ta[0]);
          if constexpr (P > 0) {
            pk[(P - 1) & 3] = o_cvt(hv[0], hv[1]);
            if constexpr (((P - 1) & 3) == 3) st128((u32x4){pk[0], pk[1], pk[2], pk[3]}, rht, vst + (((P - 1) >> 2) & 1) * 512, sof + ((P - 1) >> 3) * 4096);
          }
        }
        if constexpr (ph == 2) { ta[1] = o_exp(ta[1]); ta[0] = o_add1(ta[0]); }
        if constexpr (ph == 3) { ta[0] = o_rcp(ta[0]); ta[1] = o_add1(ta[1]); tp[0] = o_mul(ga(a), up(a)); }
        if constexpr (ph == 4) { ta[2] = o_rcp(ta[1]); tp[1] = o_mul(ga(b), up(b)); hv[0] = o_mul(tp[0], ta[0]); }
        if constexpr (G == 79) {
          asm volatile("s_nop 0");
          hv[1] = o_mul(tp[1], ta[2]); pk[3] = o_cvt(hv[0], hv[1]);
          st128((u32x4){pk[0], pk[1], pk[2], pk[3]}, rht, vst + 512, sof + 4096);
        }
        if constexpr (G >= 30 && G < 60 && (G - 30) % 5 == 2 && (G - 30) / 5 < 5) {
          const unsigned q = lane >> 4, qq = (lane & 15) >> 2, pp = lane & 3;
          const unsigned ad = tb + 8192 + (16 * (q >> 1) + 4 * (q & 1) + qq) * 160 + 8 * pp + 32 * ((G - 30) / 5);
          t0[0] = trr(ad); t1[0] = trr(ad + 1280);
        }
        if constexpr (G >= 35 && G < 60 && (G - 35) % 5 == 0) st128(__builtin_bit_cast(u32x4, cat(t0[0], t1[0])), rbw, vo, sow + 1024 * ((G - 35) / 5));
        if constexpr (G == 62) {
          const unsigned ad = tb + 8192 + 5120 + (lane & 15) * 64 + (lane >> 4) * 16;
          ca = __builtin_bit_cast(u32x4, ldsr(ad)); cb2 = __builtin_bit_cast(u32x4, ldsr(ad + 1024));
        }
        if constexpr (G == 65 || G == 70) {
          constexpr int o = G == 65 ? 0 : 2;
          const unsigned a0 = ca[o], a1 = ca[o + 1], b0 = cb2[o] ^ 0x80008000u, b1 = cb2[o + 1] ^ 0x80008000u;
          st128((u32x4){__builtin_amdgcn_perm(b0, a0, 0x05040100u), __builtin_amdgcn_perm(b0, a0, 0x07060302u),
                        __builtin_amdgcn_perm(b1, a1, 0x05040100u), __builtin_amdgcn_perm(b1, a1, 0x07060302u)}, rbc, vo, soc + (o >> 1) * 5120);
        }
      });
  }
  STAMP(2);
  auto flag_ok = [&](u32x2 f, bool need) { return __all(!need || (f[0] == tag[0] && f[1] == tag[1])); };
  auto spin = [&](unsigned off, bool need) {
#pragma unroll 1
    for (unsigned spins = 0; spins < (1u << 20); ++spins) {
      if (flag_ok(ld64_sync(rfl, off), need)) break;
    }
  };
  const int mt = 2 * rb + (idx & 1), nt = 4 * (xcd & 1) + (idx >> 1);
  const unsigned fa_off = FA_ + (lane < 16 ? rb * 16 + lane : ((lane - 16) & 31) * 8 + nt) * 128;
  const RSRC ra = mkrs(p.ws + HT_OFF + (size_t)mt * KS2 * 4096, KS2 * 4096);
  const RSRC rbb = mkrs(p.ws + BT_OFF + (size_t)nt * KS2 * 5120, KS2 * 5120);
  constexpr int NS = 24, DEP = 5, PRE = PRE_T + PRE_E;
  bf16x8 fa[DEP][4], fb[DEP][5];
  const unsigned voz = vo | ssel(mW, 0u, OOB);
  auto ld = [&](int s, int g) {
    const unsigned ks = w + 4 * s, va = s == 13 ? voz : vo;
    if (g < 4) fa[s % DEP][g] = __builtin_bit_cast(bf16x8, __builtin_amdgcn_raw_buffer_load_b128(ra, va + g * 1024, ks * 4096, SC1));
    else if (g < 8) fb[s % DEP][g - 4] = __builtin_bit_cast(bf16x8, __builtin_amdgcn_raw_buffer_load_b128(rbb, va + (g - 4) * 1024, ks * 5120, SC1));
    else fb[s % DEP][4] = __builtin_bit_cast(bf16x8, __builtin_amdgcn_raw_buffer_load_b128(rbb, va, ks * 5120 + 4096, SC1));
  };
  unsigned tch[2];
  auto touch = [&](int i) {
    if (i == 0) tch[0] = __builtin_amdgcn_raw_buffer_load_b32(ra, lane * 128, ((idx >> 1) * 4 + w) * 8192, SC1);
    else tch[1] = __builtin_amdgcn_raw_buffer_load_b32(rbb, lane * 128, 10240 + ((((r & 3) * 2 + (idx & 1)) * 4 + w) * 8192), SC1);
  };
  const unsigned fs_off = lane < 16 ? FS_ + (cg * 16 + lane) * 128 : FG_ + rb * 128;
  const bool fs_need = lane >= 16 || lane < rb;
  const RSRC rg = mkrs(p.ws + G_OFF, G_BYTES);
  u32x4 gq;
  constexpr unsigned GL = 12288, DL = 13312;
  auto ld_g = [&]() { gq = __builtin_amdgcn_raw_buffer_load_b128(rg, lane < 32 ? (unsigned)(128 * rb + 4 * lane) * 4u : OOB, 0, SC1); };
  const int stb = ((rb - 1 - 8 * h) * 640 + l32) * 8;
  auto st_off = [&](int blk, int u) { return (unsigned)max(stb - 5120 * u, -4096 * 2) + 256u * blk; };
  const unsigned no2 = ssel(m2, 0u, OOB);
  const unsigned cb = (l32 >> 2) ^ (2 * h);
  auto tw_ad = [&](int u) { return tb + h * 512 + ((cb ^ ((u & 1) + 4 * (u >> 1))) << 4) + (l32 & 3) * 4; };
  auto tr_ad = [&](int rt, int kk) { const unsigned row = lane & 15, q = lane >> 4; return tb + (16 * rt + row) * 128 + (((2 * q + kk) ^ ((row >> 1) & 7)) << 4); };
  u32x2 sv[8];
  float nr, ni, qr, qi;
  zr[1] = zr[0]; zi[1] = zi[0]; par[1] = par[0];

  if (__builtin_expect(heavy, 1)) {
    f32x16 acc[4];
    float sr, si, u0, u1;
    f32x2 sg[8];
    u32x2 fav, fsv;
    d_body<false, 4 + 2 * TOUCH + (14 * PRE_T + 49) / 50, 2 * TOUCH + (30 * PRE_T + 49) / 50>(xf, acc, st, c, rbd, sa, 0u, [&](Stream&) {},
      [&](auto NN) {
        constexpr int n = NN;
        if constexpr (n == 2) st64(tag, rfl, vlead + FA_ + (rb * 16 + cg) * 128);
        if constexpr (n == 5) st64(tag, rfl, vlead + FS_ + (cg * 16 + rb) * 128);
        if constexpr (n == 7) {
          asm volatile("s_waitcnt vmcnt(4)" : "+v"(fav)::"memory");
          if (__builtin_expect(!flag_ok(fav, true), 0)) spin(fa_off, true);
          asm volatile("" ::: "memory");
        }
      },
      [&](auto GG) {
        constexpr int G = GG;
        if constexpr (G == 0) { sr = zr[0][0]; si = zi[0][0]; }
        if constexpr (G < 22) {
          constexpr int j = (G >> 1) + 1;
          if constexpr ((G & 1) == 0) { u0 = o_fnma(par[0].li, si, zr[0][j]); u1 = o_fma(par[0].li, sr, zi[0][j]); }
          else { sr = o_fma(par[0].lr, sr, u0); si = o_fma(par[0].lr, si, u1); }
        } else if constexpr (G < 26) {
          u0 = o_fnma(par[0].li, si, zr[0][G - 10]); u1 = o_fma(par[0].li, sr, zi[0][G - 10]);
          sr = o_fma(par[0].lr, sr, u0); si = o_fma(par[0].lr, si, u1);
          if constexpr (G == 25) *(LAS f32x2*)(size_t)segw = (f32x2){sr, si};
        }
        if constexpr (G >= 28 && G < 32) { sg[2 * (G - 28)] = *(const LAS f32x2*)(size_t)(segr + 512 * (G - 28)); sg[2 * (G - 28) + 1] = *(const LAS f32x2*)(size_t)(segr + 512 * (G - 28) + 256); }
        if constexpr (G == 30) { sr = sg[0][0]; si = sg[0][1]; }
        if constexpr (G >= 30 && G < 44) {
          constexpr int j = ((G - 30) >> 1) + 1;
          if constexpr ((G & 1) == 0) { u0 = o_fnma(par[0].mi, si, sg[j][0]); u1 = o_fma(par[0].mi, sr, sg[j][1]); }
          else { sr = o_fma(par[0].mr, sr, u0); si = o_fma(par[0].mr, si, u1); }
        }
        if constexpr (G == 43) st64((u32x2){__float_as_uint(sr), __float_as_uint(si)}, rst, (unsigned)(rb * 640 + 32 * cg + l32) * 8u | (threadIdx.x < 32 ? 0u : OOB));
        if constexpr (G >= 48 && G < 60) {
          if constexpr (G == 48) { nr = par[0].mr; ni = par[0].mi; }
          if constexpr (G == 54) { qr = nr; qi = ni; }
          float& a = G < 54 ? nr : qr; float& b = G < 54 ? ni : qi;
          if constexpr ((G & 1) == 0) { u0 = o_mul(b, b); u1 = o_mul(a, b); }
          else { a = o_fms(a, a, u0); b = o_add(u1, u1); }
        }
        if constexpr (G == 89) fav = ld64_async(rfl, fa_off);
        if constexpr (TOUCH && (G == 108 || G == 109)) touch(G - 108);
        if constexpr (G >= 110 && PRE_T > 0) { constexpr int i = ((G - 110) * PRE_T + 49) / 50; if constexpr (i < PRE_T && (50 * i) / (PRE_T ? PRE_T : 1) == G - 110) ld(i / 9, i % 9); }
        if constexpr (G == 111) fsv = __builtin_amdgcn_raw_buffer_load_b64(rfl, fs_off, 0, SC1);
        if constexpr (G == 124) { if (__builtin_expect(!flag_ok(fsv, fs_need), 0)) spin(fs_off, fs_need); asm volatile("" ::: "memory"); }
        if constexpr (G == 125) ld_g();
        if constexpr (G >= 128 && G < 152 && G % 3 == 2) sv[(G - 128) / 3] = __builtin_amdgcn_raw_buffer_load_b64(rst, st_off(cg, (G - 128) / 3), 0, SC1);
      },
      [&](auto) {});
    STAMP(3);
    const unsigned sof = mtbase + (unsigned)(32 + 2 * cg) * 4096u;
    float e[8];
    sfor<12>([&](auto HH) {
      constexpr int H = HH / 3, ph = HH % 3, t = H >> 1, o = 8 * (H & 1);
      if constexpr (ph == 0) {
#pragma unroll
        for (int u = 0; u < 8; ++u) e[u] = __builtin_amdgcn_exp2f(acc[t][o + u] * -1.44269504f);
      } else if constexpr (ph == 1) {
#pragma unroll
        for (int u = 0; u < 8; ++u) e[u] = __builtin_amdgcn_rcpf(1.f + e[u]);
      } else {
        unsigned pk[4];
#pragma unroll
        for (int u = 0; u < 8; u += 2) pk[u >> 1] = cvtpk(acc[t][o + u] * acc[2 + t][o + u] * e[u], acc[t][o + u + 1] * acc[2 + t][o + u + 1] * e[u + 1]);
        st128((u32x4){pk[0], pk[1], pk[2], pk[3]}, rht, vst + (H & 1) * 512, sof + t * 4096);
      }
#pragma unroll
      for (int i = PRE_T + HH * PRE_E / 12; i < PRE_T + (HH + 1) * PRE_E / 12; ++i) ld(i / 9, i % 9);
      SB();
    });
  } else {
    if (two) s_body(xf, zr[1], zi[1], st, c, sa, [&](Stream& s) { stream_null(s, c); },
                    [&](auto NN) { if constexpr (NN == 2) st64(tag, rfl, vlead + FA_ + (rb * 16 + cg) * 128); },
                    [&](auto GG) { if constexpr (GG == 4) { par[1] = ssm_par(raw[1][0], raw[1][1], raw[1][2]); pin_par(par[1]); } });
    else {
      setm0<0>(tb); dma<0>(vo, mkrs(p.gw, 1280), 0); dma<1024>(vo, mkrs(p.gw, 1280), 0);
      wait_bar<2>();
      st64(tag, rfl, vlead + FA_ + (rb * 16 + cg) * 128);
      wait_vm<1>();
      f32x16 a;
      unsigned gk = c.rho * 256u;
#pragma unroll
      for (int n = 0; n < 5; ++n) {
        const unsigned ad = tb + gk + 16 * h;
        bf16x8 gf[8];
#pragma unroll
        for (int s = 0; s < 8; ++s) gf[s] = ldsr(ad + 32 * s);
#pragma unroll
        for (int s = 0; s < 8; ++s) { if (n + s == 0) mf_xb0(a, gf[s], xf[0]); else mf_xb(a, gf[s], xf[8 * n + s]); }
        gk = gk == 1024u ? 0u : gk + 256u;
      }
      asm volatile("s_nop 7\n\ts_nop 7" : "+v"(a));
      const float lg = rbf(a[0]);
      const float g = rbf(1.f / (1.f + __expf(-lg)));
      __builtin_amdgcn_raw_buffer_store_b32(__float_as_uint(g), rg, h == 0 ? (unsigned)(128 * rb + 32 * w + 16 * ((l32 >> 2) & 1) + 4 * (l32 >> 3) + (l32 & 3)) * 4u : OOB, 0, SC1);
      wait_bar<0>();
      st64(tag, rfl, vlead + FG_ + rb * 128);
    }
    STAMP(3);
    BST(32);
    float sr[2], si[2], u0[2], u1[2];
#pragma unroll
    for (int i = 0; i < 2; ++i) { sr[i] = zr[i][0]; si[i] = zi[i][0]; }
#pragma unroll
    for (int j = 1; j < 16; ++j) {
#pragma unroll
      for (int i = 0; i < 2; ++i) { u0[i] = o_fnma(par[i].li, si[i], zr[i][j]); u1[i] = o_fma(par[i].li, sr[i], zi[i][j]); }
#pragma unroll
      for (int i = 0; i < 2; ++i) { sr[i] = o_fma(par[i].lr, sr[i], u0[i]); si[i] = o_fma(par[i].lr, si[i], u1[i]); }
    }
#pragma unroll
    for (int i = 0; i < 2; ++i) *(LAS f32x2*)(size_t)(segw + i * 2048) = (f32x2){sr[i], si[i]};
    asm volatile("s_waitcnt lgkmcnt(0)\n\ts_barrier" ::: "memory");
    f32x2 sg[2][8];
#pragma unroll
    for (int g = 0; g < 8; ++g)
#pragma unroll
      for (int i = 0; i < 2; ++i) sg[i][g] = *(const LAS f32x2*)(size_t)(segr + i * 2048 + g * 256);
    SB();
#pragma unroll
    for (int i = 0; i < 2; ++i) { sr[i] = sg[i][0][0]; si[i] = sg[i][0][1]; }
#pragma unroll
    for (int g = 1; g < 8; ++g) {
#pragma unroll
      for (int i = 0; i < 2; ++i) { u0[i] = o_fnma(par[i].mi, si[i], sg[i][g][0]); u1[i] = o_fma(par[i].mi, sr[i], sg[i][g][1]); }
#pragma unroll
      for (int i = 0; i < 2; ++i) { sr[i] = o_fma(par[i].mr, sr[i], u0[i]); si[i] = o_fma(par[i].mr, si[i], u1[i]); }
    }
#pragma unroll
    for (int i = 0; i < 2; ++i)
      st64((u32x2){__float_as_uint(sr[i]), __float_as_uint(si[i])}, rst, (unsigned)(rb * 640 + 32 * (i ? blk1 : cg) + l32) * 8u | (threadIdx.x < 32 ? 0u : OOB));
    u32x2 fav = ld64_async(rfl, fa_off);
    nr = par[0].mr; ni = par[0].mi; csq(nr, ni); csq(nr, ni); csq(nr, ni);
    qr = nr; qi = ni; csq(qr, qi); csq(qr, qi); csq(qr, qi);
    BST(33);
    asm volatile("s_waitcnt vmcnt(0)" : "+v"(fav), "+v"(nr), "+v"(ni), "+v"(qr), "+v"(qi)::"memory");
    st64(tag, rfl, vlead + FS_ + (cg * 16 + rb) * 128);
    BST(34);
    if (bid == 249 && w == 0) {
#pragma unroll 1
      for (int u = 0; u < 4; ++u) spin(FA_ + (64 * u + lane) * 128, true);
      const u64 e1 = ep + 1;
      st64((u32x2){(unsigned)e1, (unsigned)(e1 >> 32)}, rfl, vlead + EP_);
    }
    BST(35);
    if (__builtin_expect(!flag_ok(fav, true), 0)) spin(fa_off, true);
    asm volatile("" ::: "memory");
    BST(36);
    if (TOUCH) { touch(0); touch(1); }
#pragma unroll
    for (int g = 0; g < PRE - 9; ++g) ld(g / 9, g % 9);
    const u32x2 fsv = __builtin_amdgcn_raw_buffer_load_b64(rfl, fs_off, 0, SC1);
#pragma unroll
    for (int g = PRE - 9; g < PRE; ++g) ld(g / 9, g % 9);
    BST(37);
    if (__builtin_expect(!flag_ok(fsv, fs_need), 0)) spin(fs_off, fs_need);
    asm volatile("" ::: "memory");
#pragma unroll
    for (int u = 0; u < 8; ++u) sv[u] = __builtin_amdgcn_raw_buffer_load_b64(rst, st_off(cg, u), 0, SC1);
    ld_g();
    asm volatile("fused_side_end:");
  }
  STAMP(5);
  BST(60);

  {
    f32x4 acc[4][5];
    u32x2 fz;
    float gv[4];
    f32x4 gt[4];
    f32x2 sg[7];
    float cr, ci, u0, u1, e0, e1;
    bf16x8 tf[4];
    unsigned twa[4], orow[5], oad[5], xrow;
    u32x2 xv, dv;
    float gm;
    f32x4 sk[5];
    auto tau = [](int i) { return (i & 32) + 16 * ((i >> 2) & 1) + 4 * ((i >> 3) & 3) + (i & 3); };
    LAS f32x4* red = (LAS f32x4*)smem;
    const int pw = ((lane + 4 * (lane >> 4)) & 15) + (lane & 48);
    SB();
    sfor<NS * 10>([&](auto QQ) {
      constexpr int Q = QQ, s = Q / 10, g = Q % 10;
      constexpr int R_ = 216 - PRE, n1 = PRE + (Q * R_) / QL, n0 = Q ? PRE + ((Q - 1) * R_) / QL : PRE - 1;
      constexpr bool go = n1 > n0 && n1 < 216;
      if constexpr (g == 0) {
        BST(64 + s);
        if constexpr (s == 2) {
          wait_vm<16>(); BST(90); wait_bar<16>(); BST(91);
          st64(tag, rfl, vlead + FC_ + (rb * 16 + cg) * 128);
          if (TOUCH) asm volatile("" ::"v"(tch[0]), "v"(tch[1]));
        }
        if constexpr (s == 8) {
          wait_vm<16>(); BST(92); wait_bar<16>(); BST(93);
          st64(tag, rfl, vlead + FB_ + (rb * 16 + cg) * 128);
        }
        SB();
      }
      if constexpr (Q == 146) { wait_bar<16>(); st64(tag, rfl, (vlead | no2) + FD_ + (rb * 16 + cg) * 128); }
      if constexpr (Q == 180) fz = __builtin_amdgcn_raw_buffer_load_b64(rfl, FD_ + (rb * 16 + (lane & 15)) * 128, 0, SC1);
      if constexpr (Q == 32) fz = __builtin_amdgcn_raw_buffer_load_b64(rfl, FC_ + (rb * 16 + (lane & 15)) * 128, 0, SC1);
      if constexpr (Q == 96) fz = __builtin_amdgcn_raw_buffer_load_b64(rfl, FB_ + (rb * 16 + (lane & 15)) * 128, 0, SC1);
      if constexpr (go && (n1 == 72 || n1 == 126 || n1 == 198)) {
        const unsigned o = (n1 == 72 ? FC_ : (n1 == 126 ? FB_ : FD_)) + (rb * 16 + (lane & 15)) * 128;
        const bool need = n1 == 126 || (n1 == 72 ? (lane & 15) < 11 : ((lane & 15) >= 11 && (lane & 15) < 15));
        BST(n1 == 72 ? 94 : (n1 == 126 ? 96 : 98));
        if (__builtin_expect(!flag_ok(fz, need), 0)) spin(o, need);
        asm volatile("" ::: "memory");
        BST(n1 == 72 ? 95 : (n1 == 126 ? 97 : 99));
        SB();
      }
      auto mm = [&](auto U) {
        constexpr int q = Q >= 220 ? Q - 220 : 2 * g + U, m = q / 5, n = q % 5, sl = (Q >= 220 ? 22 + U : s) % DEP;
        if constexpr (s == 0) mf16_0(acc[m][n], fb[0][n], fa[0][m]); else mf16(acc[m][n], fb[sl][n], fa[sl][m]);
      };
      if constexpr (Q >= 225) red[(w * 20 + Q - 225) * 64 + pw] = acc[(Q - 225) / 5][(Q - 225) % 5];
      mm(IC<0>{});
      SB();
      if constexpr (Q >= 135 && Q < 145) {
        sfor<2>([&](auto U) {
          constexpr int q = 2 * (Q - 135) + U, m = q / 5, n = q % 5;
#pragma unroll
          for (int e = 0; e < 4; ++e) acc[m][n][e] = o_mul(acc[m][n][e], gv[m]);
        });
      }
      if constexpr (Q >= 120 && Q < 125) {
        constexpr int n = Q - 120;
        const unsigned i = 64 * n + lane, row = i / 20, ch = i - 20 * row;
        orow[n] = row | (ch << 8); PINV(orow[n]);
      }
      if constexpr (Q >= 150 && Q < 159) {
        constexpr int n = (Q - 150) / 3, ph = (Q - 150) % 3;
        if constexpr (ph == 0) { xrow = (64 * n + lane) / 10; PINV(xrow); }
        if constexpr (ph == 1) { xrow = ((unsigned)tau(16 * w + (xrow & 15)) * 1280u + (1024 * n + 16 * lane - xrow * 160)) | (xrow < 16 ? 0u : OOB); PINV(xrow); }
        if constexpr (ph == 2) { setm0<8192 + 1024 * n>(tb); dma<0>(xrow, c.rx, (unsigned)(64 * mt) * 1280u + 160u * nt); }
      }
      if constexpr (Q == 160) { setm0<DL>(tb); dma<0>(lane < 10 ? vo : OOB, mkrs(p.dsk, D_ * 2), 160u * nt); }
      if constexpr (Q >= 201 && Q < 221) {
        constexpr int n = (Q - 201) / 4, ph = (Q - 201) % 4;
        if constexpr (ph == 0) {
          const unsigned row = orow[n] & 255, ch = orow[n] >> 8, t = tau(16 * w + row);
          xv = *(const LAS u32x2*)(size_t)(tb + 8192 + 512 * n + 8 * lane); dv = *(const LAS u32x2*)(size_t)(tb + DL + 8 * ch);
          gm = *(const LAS float*)(size_t)(tb + GL + (64 * (idx & 1) + t) * 4);
          oad[n] = (t * 640 + 4 * ch) * 4u; PINV(oad[n]);
        }
        if constexpr (ph == 1) {
          const unsigned row = orow[n] & 255, ch = orow[n] >> 8;
          orow[n] = c.lds0 + ((w * 5 + (ch >> 2)) * 64 + ((row + 4 * (ch & 3)) & 15) + 16 * (ch & 3)) * 16; PINV(orow[n]);
        }
        if constexpr (ph >= 2) {
          constexpr int o = ph - 2;
          sk[n][2 * o] = gm * rbf(bf2f(xv[o] & 0xffffu) * bf2f(dv[o] & 0xffffu)); sk[n][2 * o + 1] = gm * rbf(bf2f(xv[o] >> 16) * bf2f(dv[o] >> 16));
          if constexpr (o == 1) PINV(sk[n]);
        }
      }
      if constexpr (Q == 5) *(LAS u32x4*)(size_t)(tb + GL + vo) = gq;
      if constexpr (Q >= 10 && Q < 14) gt[Q - 10] = *(const LAS f32x4*)(size_t)(tb + GL + (32 * w + 16 * h + 4 * (Q - 10)) * 4);
      if constexpr (Q >= 116 && Q < 120) gv[Q - 116] = *(const LAS float*)(size_t)(tb + GL + (64 * (idx & 1) + tau(16 * (Q - 116) + (lane & 15))) * 4);
      if constexpr (Q >= 126 && Q < 130) { gv[Q - 126] = rbf(1.f - gv[Q - 126]); PINV(gv[Q - 126]); }
      if constexpr (Q >= 44 && Q < 52) sv[Q - 44] = __builtin_amdgcn_raw_buffer_load_b64(rst, st_off(blk1, Q - 44) | no2, 0, SC1);
      if constexpr (Q >= 54 && Q < 60) {
        if constexpr (Q == 54) { nr = par[1].mr; ni = par[1].mi; }
        if constexpr (Q == 57) { qr = nr; qi = ni; }
        float& a = Q < 57 ? nr : qr; float& b = Q < 57 ? ni : qi;
        u0 = o_mul(b, b); u1 = o_mul(a, b); a = o_fms(a, a, u0); b = o_add(u1, u1);
      }
      constexpr int I = Q >= 60, F = Q - 60 * I;
      if constexpr (F < 7) {
        if constexpr (F == 0) { cr = __uint_as_float(sv[7][0]); ci = __uint_as_float(sv[7][1]); }
        sg[F] = *(const LAS f32x2*)(size_t)(segr + 2048 * I + 256 * F);
        u0 = o_fnma(ni, ci, __uint_as_float(sv[6 - F][0])); u1 = o_fma(ni, cr, __uint_as_float(sv[6 - F][1]));
        cr = o_fma(nr, cr, u0); ci = o_fma(nr, ci, u1);
      }
      if constexpr (F == 7) {
        auto a = __builtin_amdgcn_permlane32_swap(__float_as_uint(cr), __float_as_uint(cr), false, false);
        auto b = __builtin_amdgcn_permlane32_swap(__float_as_uint(ci), __float_as_uint(ci), false, false);
        cr = __uint_as_float(a[1]); ci = __uint_as_float(b[1]);
        u0 = o_fnma(qi, ci, __uint_as_float(a[0])); u1 = o_fma(qi, cr, __uint_as_float(b[0]));
        cr = o_fma(qr, cr, u0); ci = o_fma(qr, ci, u1);
      }
      if constexpr (F >= 8 && F < 15) {
        u0 = o_fnma(par[I].mi, ci, sg[F - 8][0]); u1 = o_fma(par[I].mi, cr, sg[F - 8][1]);
        u0 = o_fma(par[I].mr, cr, u0); u1 = o_fma(par[I].mr, ci, u1);
        if (F - 8 < 2 * (int)w + h) { cr = u0; ci = u1; }
        PINV(cr); PINV(ci);
        if constexpr (Q < 12) { twa[Q - 8] = tw_ad(Q - 8); PINV(twa[Q - 8]); }
      }
      if constexpr (F >= 15 && F < 47) {
        constexpr int j = (F - 15) >> 1;
        if constexpr (((F - 15) & 1) == 0) {
          u0 = o_fnma(par[I].li, ci, zr[I][j]); u1 = o_fma(par[I].li, cr, zi[I][j]);
          e0 = o_mul(par[I].cr, gt[j >> 2][j & 3]); e1 = o_mul(par[I].ci, gt[j >> 2][j & 3]);
          cr = o_fma(par[I].lr, cr, u0); ci = o_fma(par[I].lr, ci, u1);
        } else {
          u0 = o_mul(e1, ci); u1 = o_mul(e1, cr);
          u0 = o_fms(e0, cr, u0); u1 = o_fma(e0, ci, u1);
          *(LAS unsigned*)(size_t)(twa[((j & 3) >> 1) + 2 * ((j >> 2) & 1)] + ((j & 3) + 8 * (j >> 2)) * 128) = o_cvt(u0, u1);
        }
      }
      if constexpr (F == 47 || F == 48) { tf[2 * (F - 47)] = ldsr(tr_ad(F - 47, 0)); tf[2 * (F - 47) + 1] = ldsr(tr_ad(F - 47, 1)); }
      if constexpr (F >= 50 && F < 54)
        st128(__builtin_bit_cast(u32x4, tf[F - 50]), rht, I ? vo | no2 : vo, mtbase + (unsigned)(56 + 2 * (I ? blk1 : cg) + ((F - 50) & 1)) * 4096u + ((F - 50) >> 1) * 1024);
      SB();
      mm(IC<1>{});
      SB();
      if constexpr (go) ld(n1 / 9, n1 % 9);
      SB();
    });
    STAMP(7);
    BST(88);
    asm volatile("s_nop 7\n\ts_nop 7" : "+v"(acc[3][0]), "+v"(acc[3][1]), "+v"(acc[3][2]), "+v"(acc[3][3]), "+v"(acc[3][4]));
#pragma unroll
    for (int n = 0; n < 5; ++n) red[(w * 20 + 15 + n) * 64 + pw] = acc[3][n];
    asm volatile("s_waitcnt lgkmcnt(0)\n\ts_barrier" ::: "memory");
    const RSRC ro = mkrs(p.out, L_ * D_ * 4);
    f32x4 pt[5][4];
#pragma unroll
    for (int n = 0; n < 5; ++n)
#pragma unroll
      for (int u = 0; u < 4; ++u) pt[n][u] = *(const LAS f32x4*)(size_t)(orow[n] + u * 20480);
    SB();
#pragma unroll
    for (int n = 0; n < 5; ++n)
      st128(__builtin_bit_cast(u32x4, ((pt[n][0] + pt[n][1]) + (pt[n][2] + pt[n][3])) + sk[n]), ro, oad[n], (unsigned)(64 * mt * 640 + 80 * nt) * 4u);
  }
  STAMP(8);
  asm volatile("fused_code_end:");
}

extern "C" void kernel_launch(void* const* d_in, const int* in_sizes, int n_in, void* d_out, int out_size, void* d_ws, size_t ws_size, hipStream_t stream) {
  Args a;
  a.x = (const u16*)d_in[0]; a.lre = (const u16*)d_in[1]; a.lim = (const u16*)d_in[2]; a.bre = (const u16*)d_in[3]; a.bim = (const u16*)d_in[4];
  a.cre = (const u16*)d_in[5]; a.cim = (const u16*)d_in[6]; a.ldt = (const u16*)d_in[7]; a.dsk = (const u16*)d_in[8];
  a.w1 = (const u16*)d_in[10]; a.w3 = (const u16*)d_in[11]; a.w2 = (const u16*)d_in[12]; a.gw = (const u16*)d_in[13];
  a.ws = (char*)d_ws; a.out = (float*)d_out;
  (void)hipFuncSetAttribute((const void*)fused, hipFuncAttributeMaxDynamicSharedMemorySize, LDS1);
  fused<<<256, 256, LDS1, stream>>>(a);
}
```

```cpp
#include <hip/hip_runtime.h>
#include <hip/hip_bf16.h>
#include <utility>

typedef unsigned short u16;
typedef short bf16x8 __attribute__((ext_vector_type(8)));
typedef short s16x4 __attribute__((ext_vector_type(4)));
typedef float f32x2 __attribute__((ext_vector_type(2)));
typedef float f32x4 __attribute__((ext_vector_type(4)));
typedef float f32x16 __attribute__((ext_vector_type(16)));
typedef unsigned u32x2 __attribute__((ext_vector_type(2)));
typedef unsigned u32x4 __attribute__((ext_vector_type(4)));
typedef __bf16 bf2_t __attribute__((ext_vector_type(2)));
typedef unsigned long long u64;
#define LAS __attribute__((address_space(3)))
#define DI __device__ __forceinline__
#define SB() __builtin_amdgcn_sched_barrier(0)
#define RSRC __amdgpu_buffer_rsrc_t
#define RHO 3
#define PRE_T 21
#define PRE_E 33
#define CPF 1
#define CPF_ALL 0
#define TLBW 1
#define PORD 1
#define SLP 4
#define BL 1
#define TCH 1
#define TG0 91
#define QH 30
#define NH 8
#define BLEAD 2
#define ASL 4
#define QZ 30
#define QL 212
#define TOUCH 0
constexpr int SC1 = 16;
constexpr unsigned OOB = 0x7fffff00u;

constexpr int L_ = 2048, D_ = 640, P_ = 640, F_ = 1728;
constexpr int KS2 = 96;
constexpr size_t HT_OFF = 0, HT_BYTES = (size_t)32 * KS2 * 4096;
constexpr size_t BT_OFF = HT_OFF + HT_BYTES, BT_BYTES = (size_t)8 * KS2 * 5120;
constexpr size_t G_OFF = BT_OFF + BT_BYTES, G_BYTES = 2048 * 4;
constexpr size_t ST_OFF = G_OFF + G_BYTES, ST_BYTES = 17 * 640 * 16;
constexpr size_t FL_OFF = ST_OFF + ST_BYTES;
constexpr unsigned FA_ = FL_OFF, FZ_ = FL_OFF + 32768, FG_ = FL_OFF + 65536, EP_ = FL_OFF + 67584, FL_BYTES = 67712;
constexpr unsigned WS_USED = FL_OFF + FL_BYTES;
constexpr size_t STAMP_OFF = FL_OFF + 262144;

constexpr unsigned RING = 0, XST = 81920, SCR = 147456, LDS1 = 163840;

template <int B> DI unsigned lt_mask(int a) { unsigned m; asm("s_sub_i32 %0, %1, %2\n\ts_ashr_i32 %0, %0, 31" : "=s"(m) : "s"(a), "n"(B) : "scc"); return m; }
DI unsigned ssel(unsigned m, unsigned a, unsigned b) { return b ^ ((a ^ b) & m); }
DI const void* psel(unsigned m, const void* a, const void* b) { return (const void*)((u64)b ^ (((u64)a ^ (u64)b) & (((u64)m << 32) | m))); }
template <int K> DI void wlane(unsigned& t, unsigned x) { asm("v_writelane_b32 %0, %1, %2" : "+v"(t) : "s"(x), "n"(K)); }
DI unsigned cvtpk(float lo, float hi) { f32x2 v = {lo, hi}; bf2_t b = __builtin_convertvector(v, bf2_t); return __builtin_bit_cast(unsigned, b); }
DI float bf2f(unsigned v) { return __uint_as_float(v << 16); }
DI float rbf(float x) { return __uint_as_float(cvtpk(x, 0.f) << 16); }
DI s16x4 trr(unsigned a) { return __builtin_amdgcn_ds_read_tr16_b64_v4i16((LAS s16x4*)(size_t)a); }
DI bf16x8 ldsr(unsigned a) { return *(const LAS bf16x8*)(size_t)a; }
DI bf16x8 cat(s16x4 a, s16x4 b) { return __builtin_shufflevector(a, b, 0, 1, 2, 3, 4, 5, 6, 7); }
template <int IMM> DI void dma(unsigned voff, RSRC rs, unsigned soff) {
  asm volatile("buffer_load_dwordx4 %0, %1, %2 offen offset:%3 lds" ::"v"(voff), "s"(rs), "s"(soff), "n"(IMM) : "memory");
}
template <int IMM> DI void dma2(unsigned voff, RSRC rs, unsigned s0, unsigned s1) {
  unsigned t;
  asm volatile("s_add_u32 %0, %3, %4\n\tbuffer_load_dwordx4 %1, %2, %0 offen offset:%5 lds" : "=&s"(t) : "v"(voff), "s"(rs), "s"(s0), "s"(s1), "n"(IMM) : "memory", "scc");
}
template <int IMM, int ADD> DI void dma2i(unsigned voff, RSRC rs, unsigned s0) {
  unsigned t;
  asm volatile("s_add_u32 %0, %3, %4\n\tbuffer_load_dwordx4 %1, %2, %0 offen offset:%5 lds" : "=&s"(t) : "v"(voff), "s"(rs), "s"(s0), "n"(ADD), "n"(IMM) : "memory", "scc");
}
template <int IMM> DI void setm0(unsigned v) { asm volatile("s_add_u32 m0, %0, %1\n\ts_nop 0" ::"s"(v), "n"(IMM) : "memory", "scc"); }
#define PINS(x) asm volatile("" : "+s"(x))
template <int N> DI void wait_bar() { asm volatile("s_waitcnt vmcnt(%0)\n\ts_barrier" ::"n"(N) : "memory"); }
#define BST(slot)
template <int N> DI void wait_vm() { asm volatile("s_waitcnt vmcnt(%0)" ::"n"(N) : "memory"); }
DI RSRC mkrs(const void* p, unsigned bytes) { return __builtin_amdgcn_make_buffer_rsrc((void*)p, 0, bytes, 0x00020000); }
DI void st128(u32x4 v, RSRC r, unsigned vo, unsigned so) { __builtin_amdgcn_raw_buffer_store_b128(v, r, vo, so, SC1); asm volatile("s_nop 1" ::"v"(v) : "memory"); }
DI void st64(u32x2 v, RSRC r, unsigned vo, unsigned so) { __builtin_amdgcn_raw_buffer_store_b64(v, r, vo, so, SC1); }
DI u32x2 ld64_async(RSRC r, unsigned vo) { u32x2 v; asm volatile("buffer_load_dwordx2 %0, %1, %2, 0 offen sc1 ; HIDLD" : "=v"(v) : "v"(vo), "s"(r) : "memory"); return v; }
DI u32x2 ld64_sync(RSRC r, unsigned vo) { u32x2 v; asm volatile("buffer_load_dwordx2 %0, %1, %2, 0 offen sc1\n\ts_waitcnt vmcnt(0)" : "=&v"(v) : "v"(vo), "s"(r) : "memory"); return v; }
#define PINV(x) asm volatile("" : "+v"(x))
#define PINA(x) asm volatile("" : "+a"(x))
#define MFMA16(a, b, c) __builtin_amdgcn_mfma_f32_16x16x32_bf16((a), (b), (c), 0, 0, 0)
#define ALL_AGPRS "a0", "a1", "a2", "a3", "a4", "a5", "a6", "a7", "a8", "a9", "a10", "a11", "a12", "a13", "a14", "a15", "a16", "a17", "a18", "a19", "a20", "a21", "a22", "a23", "a24", "a25", "a26", "a27", "a28", "a29", "a30", "a31", "a32", "a33", "a34", "a35", "a36", "a37", "a38", "a39", "a40", "a41", "a42", "a43", "a44", "a45", "a46", "a47", "a48", "a49", "a50", "a51", "a52", "a53", "a54", "a55", "a56", "a57", "a58", "a59", "a60", "a61", "a62", "a63", "a64", "a65", "a66", "a67", "a68", "a69", "a70", "a71", "a72", "a73", "a74", "a75", "a76", "a77", "a78", "a79", "a80", "a81", "a82", "a83", "a84", "a85", "a86", "a87", "a88", "a89", "a90", "a91", "a92", "a93", "a94", "a95", "a96", "a97", "a98", "a99", "a100", "a101", "a102", "a103", "a104", "a105", "a106", "a107", "a108", "a109", "a110", "a111", "a112", "a113", "a114", "a115", "a116", "a117", "a118", "a119", "a120", "a121", "a122", "a123", "a124", "a125", "a126", "a127", "a128", "a129", "a130", "a131", "a132", "a133", "a134", "a135", "a136", "a137", "a138", "a139", "a140", "a141", "a142", "a143", "a144", "a145", "a146", "a147", "a148", "a149", "a150", "a151", "a152", "a153", "a154", "a155", "a156", "a157", "a158", "a159", "a160", "a161", "a162", "a163", "a164", "a165", "a166", "a167", "a168", "a169", "a170", "a171", "a172", "a173", "a174", "a175", "a176", "a177", "a178", "a179", "a180", "a181", "a182", "a183", "a184", "a185", "a186", "a187", "a188", "a189", "a190", "a191", "a192", "a193", "a194", "a195", "a196", "a197", "a198", "a199", "a200", "a201", "a202", "a203", "a204", "a205", "a206", "a207", "a208", "a209", "a210", "a211", "a212", "a213", "a214", "a215", "a216", "a217", "a218", "a219", "a220", "a221", "a222", "a223", "a224", "a225", "a226", "a227", "a228", "a229", "a230", "a231", "a232", "a233", "a234", "a235", "a236", "a237", "a238", "a239", "a240", "a241", "a242", "a243", "a244", "a245", "a246", "a247", "a248", "a249", "a250", "a251", "a252", "a253", "a254", "a255"
template <int K> DI void mf_xa0(f32x16& d, const bf16x8& w) { asm volatile("v_mfma_f32_32x32x16_bf16 %0, a[%c2:%c3], %1, 0" : "=&v"(d) : "v"(w), "n"(4 * K), "n"(4 * K + 3)); }
template <int K> DI void mf_xa(f32x16& d, const bf16x8& w) { asm volatile("v_mfma_f32_32x32x16_bf16 %0, a[%c2:%c3], %1, %0" : "+v"(d) : "v"(w), "n"(4 * K), "n"(4 * K + 3)); }
template <int K> DI void mf_xb0(f32x16& d, const bf16x8& w) { asm volatile("v_mfma_f32_32x32x16_bf16 %0, %1, a[%c2:%c3], 0" : "=&v"(d) : "v"(w), "n"(4 * K), "n"(4 * K + 3)); }
template <int K> DI void mf_xb(f32x16& d, const bf16x8& w) { asm volatile("v_mfma_f32_32x32x16_bf16 %0, %1, a[%c2:%c3], %0" : "+v"(d) : "v"(w), "n"(4 * K), "n"(4 * K + 3)); }
template <int K> DI void rd_x(unsigned ad) { asm volatile("ds_read_b128 a[%c1:%c2], %0" ::"v"(ad), "n"(4 * K), "n"(4 * K + 3) : "memory"); }
template <int A, int B> DI void mf16_0(f32x4& d) { asm volatile("v_mfma_f32_16x16x32_bf16 %0, a[%c1:%c2], a[%c3:%c4], 0" : "=&v"(d) : "n"(A), "n"(A + 3), "n"(B), "n"(B + 3)); }
template <int A, int B> DI void mf16(f32x4& d) { asm volatile("v_mfma_f32_16x16x32_bf16 %0, a[%c1:%c2], a[%c3:%c4], %0" : "+v"(d) : "n"(A), "n"(A + 3), "n"(B), "n"(B + 3)); }
template <int R, int IMM> DI void ld_frag(RSRC r, unsigned vo, unsigned so) {
  asm volatile("buffer_load_dwordx4 a[%c3:%c4], %0, %1, %2 offen offset:%5 sc1" ::"v"(vo), "s"(r), "s"(so), "n"(R), "n"(R + 3), "n"(IMM) : "memory");
}
template <int ADD> DI void s_step(unsigned& x) { asm volatile("s_add_u32 %0, %0, %1" : "+s"(x) : "n"(ADD) : "scc"); }
DI void touch(unsigned& d, RSRC r, unsigned vo, unsigned so) { asm volatile("buffer_load_dword %0, %1, %2, %3 offen ; HIDLD" : "=v"(d) : "v"(vo), "s"(r), "s"(so) : "memory"); }
DI u32x4 ld128_async(RSRC r, unsigned vo, unsigned so) { u32x4 v; asm volatile("buffer_load_dwordx4 %0, %1, %2, %3 offen sc1 ; HIDLD" : "=v"(v) : "v"(vo), "s"(r), "s"(so) : "memory"); return v; }
DI void hid_ok(u32x4 (&v)[8]) { asm volatile("; HIDOK %0 %1 %2 %3 %4 %5 %6 %7" : "+v"(v[0]), "+v"(v[1]), "+v"(v[2]), "+v"(v[3]), "+v"(v[4]), "+v"(v[5]), "+v"(v[6]), "+v"(v[7])); }
DI void hid_ok(u32x4& v) { asm volatile("; HIDOK %0" : "+v"(v)); }
DI void mfma_done(f32x16& a, f32x16& b) { asm volatile("s_nop 7\n\ts_nop 7" : "+v"(a), "+v"(b)); }
DI void mfma_done4(f32x16 (&a)[4]) { asm volatile("s_nop 7\n\ts_nop 7" : "+v"(a[0]), "+v"(a[1]), "+v"(a[2]), "+v"(a[3])); }
#define STAMP(i) do {} while (0)

template <int V> using IC = std::integral_constant<int, V>;
template <class Fn, int... Is> DI void sfor_(Fn&& f, std::integer_sequence<int, Is...>) { (f(IC<Is>{}), ...); }
template <int N, class Fn> DI void sfor(Fn&& f) { sfor_(f, std::make_integer_sequence<int, N>{}); }

struct Args {
  const u16 *x, *w1, *w3, *lre, *lim, *ldt;
  char* ws;
  const u16 *gw, *bre, *bim, *cre, *cim, *dsk, *w2;
  float* out;
};

struct Stream {
  RSRC ra, rb;
  unsigned v0, v1, v2, v3;
  unsigned k, end, adv, lim, d1, d2, d3;
};
template <int Q> DI void st_piece(const Stream& s) {
  if constexpr (Q == 0) dma<0>(s.v0, s.ra, s.k);
  else if constexpr (Q == 1) dma2<1024>(s.v1, s.ra, s.k, s.d1);
  else if constexpr (Q == 2) dma2<2048>(s.v2, s.rb, s.k, s.d2);
  else dma2<3072>(s.v3, s.rb, s.k, s.d3);
}
DI void st_adv(Stream& s) { PINS(s.k); const unsigned k = s.k + s.adv; s.k = (k >= s.end) ? k - s.lim : k; }

struct Ctx {
  RSRC rx, rB, rw1, rw3, rnull;
  unsigned vS0, vS1, vS2, vS3, vD;
  unsigned lds0, m0ring;
  unsigned rho, w;
};
DI void stream_S(Stream& s, const Ctx& c, int i) {
  s.ra = c.rB; s.rb = c.rB; s.v0 = c.vS0; s.v1 = c.vS1; s.v2 = c.vS2; s.v3 = c.vS3;
  const unsigned base = (unsigned)(32 * i + 16 * (c.w & 1)) * 1280u;
  s.k = base + c.rho * 256u; s.end = base + 1280u; s.adv = 256u; s.lim = 1280u;
  s.d1 = 4096u; s.d2 = 8192u; s.d3 = 12288u;
}
DI void stream_D(Stream& s, const Ctx& c, int j) {
  s.ra = c.rw1; s.rb = c.rw3; s.v0 = s.v1 = s.v2 = s.v3 = c.vD;
  s.k = (unsigned)j * 128u + c.rho * (128u * 3456u); s.end = 640u * 3456u; s.adv = 64u * 3456u; s.lim = 640u * 3456u;
  s.d1 = 8u * 3456u - 1024u; s.d2 = 0u; s.d3 = 8u * 3456u - 1024u;
}
DI void stream_null(Stream& s, const Ctx& c) {
  s.ra = c.rnull; s.rb = c.rnull; s.k = 0; s.end = 1; s.adv = 0; s.lim = 0; s.d1 = s.d2 = s.d3 = 0;
}

struct Par { float lr, li, cr, ci, mr, mi; };
DI void csq(float& a, float& b) { const float r = a * a - b * b, i = 2.f * a * b; a = r; b = i; }
DI Par ssm_par(unsigned ulre, unsigned ulim, unsigned uldt) {
  const float Lr = bf2f(ulre), Li = bf2f(ulim);
  const float dt = rbf(expf(bf2f(uldt)));
  const float e = expf(dt * Lr), th = dt * Li;
  const float n = __builtin_rintf(th * 0.636619772f);
  float y = fmaf(n, -1.57079601287841796875f, th);
  y = fmaf(n, -3.1391647326017846353352069854736328125e-7f, y);
  y = fmaf(n, -5.390302529957764765544681040410068817436695098876953125e-15f, y);
  const float y2 = y * y;
  float ps = fmaf(y2, 2.6083159809786593541503e-06f, -0.0001981069071916863322258f);
  ps = fmaf(ps, y2, 0.00833307858556509017944336f); ps = fmaf(ps, y2, -0.166666597127914428710938f);
  const float sn = fmaf(ps * y2, y, y);
  float pc = fmaf(y2, -2.71811842367242206819355e-07f, 2.47990446951007470488548e-05f);
  pc = fmaf(pc, y2, -0.00138888787478208541870117f); pc = fmaf(pc, y2, 0.0416666641831398010253906f); pc = fmaf(pc, y2, -0.5f);
  const float cs = fmaf(pc, y2, 1.f);
  const int q = (int)n;
  float sv = (q & 1) ? cs : sn, cv = (q & 1) ? sn : cs;
  sv = (q & 2) ? -sv : sv; cv = ((q + 1) & 2) ? -cv : cv;
  Par r; r.lr = e * cv; r.li = e * sv;
  const float nr = r.lr - 1.f, ni = r.li, den = Lr * Lr + Li * Li;
  r.cr = 2.f * ((nr * Lr + ni * Li) / den); r.ci = 2.f * ((ni * Lr - nr * Li) / den);
  r.mr = r.lr; r.mi = r.li;
  csq(r.mr, r.mi); csq(r.mr, r.mi); csq(r.mr, r.mi); csq(r.mr, r.mi);
  return r;
}
DI void pin_par(Par& p) { asm volatile("" : "+v"(p.lr), "+v"(p.li), "+v"(p.cr), "+v"(p.ci), "+v"(p.mr), "+v"(p.mi)); }
DI unsigned ldu16(const u16* p, unsigned vo) { unsigned r; asm volatile("global_load_ushort %0, %1, %2 ; HIDLD" : "=v"(r) : "v"(vo), "s"(p) : "memory"); return r; }
DI float o_mul(float a, float b) { float r; asm volatile("v_mul_f32 %0, %1, %2" : "=v"(r) : "v"(a), "v"(b)); return r; }
DI float o_mulk(float a) { float r; asm volatile("v_mul_f32 %0, 0xbfb8aa3b, %1" : "=v"(r) : "v"(a)); return r; }
DI float o_add1(float a) { float r; asm volatile("v_add_f32 %0, 1.0, %1" : "=v"(r) : "v"(a)); return r; }
DI float o_exp(float a) { float r; asm volatile("v_exp_f32 %0, %1" : "=v"(r) : "v"(a)); return r; }
DI float o_rcp(float a) { float r; asm volatile("v_rcp_f32 %0, %1" : "=v"(r) : "v"(a)); return r; }
DI float o_fma(float a, float b, float c) { float r; asm volatile("v_fma_f32 %0, %1, %2, %3" : "=v"(r) : "v"(a), "v"(b), "v"(c)); return r; }
DI float o_fnma(float a, float b, float c) { float r; asm volatile("v_fma_f32 %0, -%1, %2, %3" : "=v"(r) : "v"(a), "v"(b), "v"(c)); return r; }
DI float o_fms(float a, float b, float c) { float r; asm volatile("v_fma_f32 %0, %1, %2, -%3" : "=v"(r) : "v"(a), "v"(b), "v"(c)); return r; }
DI float o_add(float a, float b) { float r; asm volatile("v_add_f32 %0, %1, %2" : "=v"(r) : "v"(a), "v"(b)); return r; }
template <int Q>
DI void seam_slot(f32x2& s, f32x2 z, f32x2 lam, f32x2 cf, f32x2 g2, f32x2 g, f32x2 u, unsigned& hz, unsigned& hf) {
  asm volatile(
      "v_pk_fma_f32 v[252:253], %4, %0, %3 op_sel:[1,1,0] op_sel_hi:[1,0,1] neg_lo:[1,0,0]\n\t"
      "v_pk_mul_f32 v[246:247], %7, %9 op_sel_hi:[1,0]\n\t"
      "v_pk_mul_f32 v[250:251], %5, %6 op_sel:[0,%c10] op_sel_hi:[1,%c10]\n\t"
      "v_pk_fma_f32 %0, %4, %0, v[252:253] op_sel_hi:[0,1,1]\n\t"
      "v_exp_f32 v246, v246\n\t"
      "v_exp_f32 v247, v247\n\t"
      "v_pk_mul_f32 v[248:249], v[250:251], %0 op_sel:[1,1] op_sel_hi:[1,0]\n\t"
      "v_pk_add_f32 v[246:247], v[246:247], 1.0 op_sel_hi:[1,0]\n\t"
      "v_pk_mul_f32 v[252:253], %7, %8\n\t"
      "v_rcp_f32 v246, v246\n\t"
      "v_rcp_f32 v247, v247\n\t"
      "v_pk_fma_f32 v[248:249], v[250:251], %0, v[248:249] op_sel_hi:[0,1,1] neg_lo:[0,0,1]\n\t"
      "v_pk_mul_f32 v[252:253], v[252:253], v[246:247]\n\t"
      "v_cvt_pk_bf16_f32 %1, v248, v249\n\t"
      "v_cvt_pk_bf16_f32 %2, v252, v253"
      : "+v"(s), "=&v"(hz), "=&v"(hf)
      : "v"(z), "v"(lam), "v"(cf), "v"(g2), "v"(g), "v"(u), "s"((u64)0xbfb8aa3bull), "n"(Q)
      : "v246", "v247", "v248", "v249", "v250", "v251", "v252", "v253");
}
DI float o_sel(float a, float b, u64 m) { float r; asm volatile("v_cndmask_b32 %0, %1, %2, %3" : "=v"(r) : "v"(a), "v"(b), "s"(m)); return r; }
DI unsigned o_cvt(float a, float b) { unsigned r; asm volatile("v_cvt_pk_bf16_f32 %0, %1, %2" : "=v"(r) : "v"(a), "v"(b)); return r; }
DI void cmad(float& a, float& b, float mr, float mi, float u, float v) {
  const float x = fmaf(mr, a, fmaf(-mi, b, u)), y = fmaf(mr, b, fmaf(mi, a, v));
  a = x; b = y;
}

template <class Next, class Bar, class Gap>
DI void s_body(f32x16& zre, f32x16& zim, Stream& st, const Ctx& c, const unsigned (&sa)[8], Next next, Bar bar, Gap gap) {
  bf16x8 wr[3], wi[3];
  auto rd = [&](int ks, int im) -> bf16x8 {
    const int n = ks >> 3, s = ks & 7;
    return n < 4 ? ldsr(sa[s] + n * 16384 + im * 8192) : ldsr(sa[s] + 65536u + im * 8192);
  };
  BST(20); wait_bar<8>(); BST(21);
  setm0<3 * 16384>(c.m0ring);
  wr[0] = rd(0, 0); wi[0] = rd(0, 1); wr[1] = rd(1, 0); wi[1] = rd(1, 1);
  st_piece<0>(st);
  SB();
  sfor<80>([&](auto GG) {
    constexpr int G = GG, ks = G >> 1, im = G & 1;
    if constexpr (im == 0 && (ks & 7) == 6 && ks < 38) {
      constexpr int n = (ks >> 3) + 1;
      BST(20 + 2 * n); wait_bar<8>(); BST(21 + 2 * n);
      bar(IC<n>{});
      if constexpr (n == 2) next(st);
      setm0<((n + 3) % 5) * 16384>(c.m0ring);
      SB();
    }
    if constexpr (ks == 0) { if constexpr (im) mf_xa0<0>(zim, wi[0]); else mf_xa0<0>(zre, wr[0]); }
    else { if constexpr (im) mf_xa<ks>(zim, wi[ks % 3]); else mf_xa<ks>(zre, wr[ks % 3]); }
    SB();
    if constexpr (im == 0 && (ks & 1) == 0 && ks <= 36) {
      constexpr int q = ((ks + 2) >> 1) & 3;
      st_piece<q>(st);
      if constexpr (q == 3) st_adv(st);
    }
    if constexpr (ks + 2 < 40) { if constexpr (im) wi[(ks + 2) % 3] = rd(ks + 2, 1); else wr[(ks + 2) % 3] = rd(ks + 2, 0); }
    gap(GG);
    SB();
  });
}

template <bool FIRST, int W8, int W9, class Next, class Bar, class Gap, class Pre>
DI void d_body(f32x16 (&acc)[4], Stream& st, const Ctx& c, const unsigned (&rbd)[4], const unsigned (&sa)[8], unsigned xso,
               Next next, Bar bar, Gap gap, Pre pre) {
  s16x4 wl[2][4], wh[2][4];
  auto rd1 = [&](int ks, int t, int half) -> s16x4 {
    const unsigned off = ((ks >> 2) % 5) * 16384 + (ks & 3) * 4096 + (t >> 1) * 2048 + half * 512;
    return trr(off >= 49152 ? rbd[2 + (t & 1)] + (off - 49152) : rbd[t & 1] + off);
  };
  const unsigned xdelta = XST + c.w * 16384u;
  unsigned xk = xso + c.rho * 256u;
  const unsigned xm0 = c.lds0 + xdelta;
  auto xpiece = [&](auto M, auto Q) {
    constexpr int m = M, q = Q;
    if constexpr (q == 0 && m > 0) { PINS(xk); xk += 256u; xk = xk >= xso + 1280u ? xk - 1280u : xk; }
    if constexpr ((q & 3) == 0) setm0<(m & 1) * 8192 + (q >> 2) * 4096>(xm0);
    constexpr int ro = (16 * (q & 1) + 4 * (q >> 1)) * 1280 - 1024 * (q & 3);
    dma2i<1024 * (q & 3), ro>((q & 3) == 0 ? c.vS0 : ((q & 3) == 1 ? c.vS1 : ((q & 3) == 2 ? c.vS2 : c.vS3)), c.rx, xk);
  };
  auto wpiece = [&](auto N, auto Q) {
    constexpr int n = N, q = Q;
    if constexpr (q == 0) { if constexpr (n == 10) next(st); setm0<(n % 5) * 16384>(c.m0ring); }
    st_piece<q>(st);
    if constexpr (q == 3) st_adv(st);
  };
  if constexpr (FIRST) {
    sfor<4>([&](auto Q) { wpiece(IC<0>{}, Q); });
    sfor<8>([&](auto Q) { xpiece(IC<0>{}, Q); });
    sfor<4>([&](auto Q) { wpiece(IC<1>{}, Q); });
    SB();
    pre(IC<-1>{});
    SB();
    pre(IC<-2>{});
    sfor<4>([&](auto Q) { xpiece(IC<1>{}, Q); });
    pre(IC<-3>{});
    SB();
  }
  BST(FIRST ? 0 : 40); wait_bar<FIRST ? 13 : 8>(); BST(FIRST ? 1 : 41);
  bar(IC<0>{});
  if constexpr (FIRST) {
    sfor<8>([&](auto S) { rd_x<S>(sa[S] + xdelta); });
    SB();
  } else setm0<3 * 16384>(c.m0ring);
#pragma unroll
  for (int t = 0; t < 4; ++t) { wl[0][t] = rd1(0, t, 0); wh[0][t] = rd1(0, t, 1); }
  if constexpr (!FIRST) st_piece<0>(st);
  SB();
  sfor<160>([&](auto GG) {
    constexpr int G = GG, ks = G >> 2, t = G & 3;
    if constexpr (t == 0 && (ks & 3) == 3 && ks < 39) {
      constexpr int n = (ks >> 2) + 1;
      BST((FIRST ? 0 : 40) + 2 * n);
      wait_bar<FIRST ? ((n & 1) ? 10 : 6) : (n == 8 ? W8 : (n == 9 ? W9 : 8))>();
      BST((FIRST ? 1 : 41) + 2 * n);
      bar(IC<n>{});
      if constexpr (!FIRST && n < 7) setm0<((n + 3) % 5) * 16384>(c.m0ring);
      SB();
    }
    const bf16x8 wf = cat(wl[ks & 1][t], wh[ks & 1][t]);
    if constexpr (ks == 0) mf_xb0<0>(acc[t], wf); else mf_xb<ks>(acc[t], wf);
    SB();
    if constexpr (FIRST) {
      if constexpr ((t & 1) == 0) {
        constexpr int s = G >> 1;
        if constexpr (s < 4) xpiece(IC<1>{}, IC<4 + s>{});
        else if constexpr (s < 60) {
          constexpr int u = (s - 4) % 16, m = (s - 4) / 16;
          if constexpr (u < 8) wpiece(IC<2 + 2 * m + (u >> 2)>{}, IC<u & 3>{}); else xpiece(IC<2 + m>{}, IC<u - 8>{});
        } else if constexpr (s < 68) wpiece(IC<10 + ((s - 60) >> 2)>{}, IC<s & 3>{});
        else if constexpr (s < 70) pre(IC<s - 68>{});
        else if constexpr (s < 74) wpiece(IC<12>{}, IC<s - 70>{});
        else if constexpr (s < 79) pre(IC<s - 72>{});
      }
      constexpr int g8 = G - 28;
      if constexpr (g8 >= 0 && (g8 & 31) < 8 && (g8 >> 5) < 4) rd_x<(8 * ((g8 >> 5) + 1) + (g8 & 7)) % 40>(sa[g8 & 7] + xdelta + (((g8 >> 5) + 1) & 1) * 8192);
    } else if constexpr (t == 0 && ks < 27) {
      st_piece<(ks + 1) & 3>(st);
      if constexpr (((ks + 1) & 3) == 3) st_adv(st);
    }
    if constexpr (ks < 39) { wl[(ks + 1) & 1][t] = rd1(ks + 1, t, 0); wh[(ks + 1) & 1][t] = rd1(ks + 1, t, 1); }
    gap(GG);
    SB();
  });
  mfma_done4(acc);
}

struct LdOrd {
  signed char s[216], k[216]; int firstA[24], last[24];
};
constexpr LdOrd make_ord() {
  LdOrd o{};
  int n = 0, nb = 0;
  auto A = [&](int s) { o.firstA[s] = n; for (int k = 5; k < 9; ++k) { o.s[n] = s; o.k[n] = k; o.last[s] = n; ++n; } };
  auto B = [&]() { for (int k = 0; k < 5; ++k) { o.s[n] = nb; o.k[n] = k; ++n; } ++nb; };
  for (int s = 0; s < 24; ++s) {
    while (nb <= s) B();
    if (s == 8) for (int j = 0; j < BLEAD; ++j) B();
    A(s);
  }
  return o;
}
constexpr LdOrd kOrd = make_ord();
constexpr int ld_sched(int q, int pre) { return q < QH ? pre + (q * NH) / QH : pre + NH + ((q - QH) * (216 - pre - NH)) / (QL - QH); }

__global__ __launch_bounds__(256, 1) void fused(Args p0) {
  Args p;
  p.x = p0.x; p.w1 = p0.w1; p.w3 = p0.w3; p.lre = p0.lre; p.lim = p0.lim; p.ldt = p0.ldt; p.ws = p0.ws; p.gw = p0.gw;
  extern __shared__ __attribute__((aligned(16))) char smem[];
  const int lane = threadIdx.x & 63;
  const unsigned w = __builtin_amdgcn_readfirstlane(threadIdx.x >> 6);
  const int bid = blockIdx.x, xcd = bid & 7, r = bid >> 3, idx = r >> 2;
  const int rb = (xcd >> 1) * 4 + (r & 3), cg = 2 * idx + (xcd & 1);
  STAMP(0);
  Ctx c;
  c.w = w; c.rho =
    (unsigned)(rb + idx) % 3u * 2u;
  c.lds0 = (unsigned)(size_t)smem; c.m0ring = c.lds0 + RING + w * 4096u;
  BST(62);
  const int l32 = lane & 31, h = lane >> 5;
  const bool heavy = cg < 11, two = cg >= 11 && cg < 15;
  const unsigned mH = lt_mask<11>(cg), m2 = lt_mask<15>(cg) & ~mH, mW = lt_mask<2>(w);
  const int blk1 = __builtin_amdgcn_readfirstlane(cg + (5 & m2));
  unsigned raw0[3];
  { const unsigned ch = (32 * cg + l32) * 2; raw0[0] = ldu16(p.lre, ch); raw0[1] = ldu16(p.lim, ch); raw0[2] = ldu16(p.ldt, ch); }
  unsigned cpf, raw[3];
  const unsigned* tlp;
  c.rx = mkrs(p.x, L_ * D_ * 2);
  c.rw1 = mkrs(p.w1, D_ * F_ * 2); c.rw3 = mkrs((const char*)p.w3 - 2048, D_ * F_ * 2 + 2048); c.rnull = mkrs(p.x, 0);
  {
    const unsigned rr = lane >> 4, t = (lane & 15) ^ rr;
    c.vS0 = rr * 1280 + t * 16; c.vS1 = rr * 1280 + (t ^ 4) * 16; c.vS2 = rr * 1280 + (t ^ 8) * 16; c.vS3 = rr * 1280 + (t ^ 12) * 16;
    const unsigned lrow = lane >> 3, lch = (lane & 7) ^ (((lrow >> 1) & 1) << 2);
    c.vD = (lrow + 16 * w) * 3456 + lch * 16;
  }
  u64 ep = *(const __attribute__((address_space(4))) u64*)(size_t)(p.ws + EP_);
  asm volatile("" ::: ALL_AGPRS);
  Par par[2];
  Stream st;
  stream_D(st, c, cg);
  unsigned sa[8], rbd[4];
  const unsigned tb = c.lds0 + XST + w * 16384;
  int v = ((rb * 16 + cg) >> 3) * 4 + w, ft, ntw, ci_, ct;
  unsigned hasw, hasc;

  u32x2 tag;
  const RSRC rws = mkrs(p.ws, WS_USED);
  unsigned abase, bbase, myfl;
  unsigned mtbase, vlead, segw, segr, vst, vo, fa_off, stv, tw0, trd;
  int stb;
  const int mt = 2 * rb + (idx & 1), nt = 4 * (xcd & 1) + (idx >> 1);
  unsigned tch[3];
  Stream st2;
  unsigned sow, soc, vow, voc;
  f32x16 accp[4];
  {
    unsigned pv[6];
    d_body<true, 0, 0>(accp, st, c, rbd, sa, (unsigned)(128 * rb + 32 * w) * 1280u,
      [&](Stream& s) { PINS(c.rho); stream_S(s, c, cg); },
      [&](auto) {},
      [&](auto GG) {
        constexpr int G = GG;
        if constexpr (G == 13) {
          asm volatile("; HIDOK %0 %1 %2 %3 %4" : "+v"(raw[0]), "+v"(raw[1]), "+v"(raw[2]), "+v"(cpf), "+v"(tlp));
          if (TLBW) asm volatile("global_load_dword %0, %1, off ; HIDLD" : "=v"(cpf) : "v"(tlp) : "memory");
        }
        if constexpr (G == 41) { PINS(ep); const u64 t = ep * 0x9E3779B97F4A7C15ull + 0x1234567ull; tag = (u32x2){(unsigned)t, (unsigned)(t >> 32)}; PINV(tag); }
        if constexpr (G == 49) { int t = mt; PINS(t); abase = HT_OFF + ((unsigned)t * KS2 + w) * 4096u; PINS(abase); }
        if constexpr (G == 53) { int t = nt; PINS(t); bbase = BT_OFF + ((unsigned)t * KS2 + w) * 5120u; PINS(bbase); }
        if constexpr (G == 57) { myfl = (rb * 16 + cg) * 128; PINS(myfl); }
        if constexpr (G == 69) { mtbase = (unsigned)((2 * rb + (w >> 1)) * KS2 * 4 + 2 * (w & 1)) * 1024u; PINS(mtbase); vo = lane * 16; PINV(vo); vlead = threadIdx.x == 0 ? 0u : OOB; PINV(vlead); }
        if constexpr (G == 73) { segr = c.lds0 + SCR + l32 * 8; PINV(segr); segw = segr + (2 * w + h) * 256; PINV(segw); }
        if constexpr (G == 77) { vst = (unsigned)((l32 >> 4) * 1024 + h * 256 + (l32 & 15) * 16); PINV(vst); }
        if constexpr (G == 81) {
          unsigned a = rb * 16 + lane, b = (lane - 16) * 8 + nt; PINV(a); PINV(b);
          fa_off = FA_ + (lane < 16 ? a : b) * 128; PINV(fa_off);
        }
        if constexpr (G == 85) { fa_off = lane < 48 ? fa_off : FG_ + rb * 128; PINV(fa_off); }
        if constexpr (G == 89) { stb = ((rb - 8 * h) * 640 + l32) * 16; PINV(stb); }
        if constexpr (G == 97) { stv = (unsigned)((h ? 0 : rb + 1) * 640 + l32) * 16u | (threadIdx.x < (rb ? 32 : 64) ? 0u : OOB); PINV(stv); }
        if constexpr (G == 113) { tw0 = tb + h * 512 + (((l32 >> 2) ^ (2 * h)) << 4) + (l32 & 3) * 4; PINV(tw0); }
        if constexpr (G == 117) { trd = tb + (lane & 15) * 128 + (((2 * (lane >> 4)) ^ ((lane >> 1) & 7)) << 4); PINV(trd); }
        if constexpr ((TCH & 1) && G == TG0) {
          const unsigned i = (w & 1) * 40 + lane, row = i / 10;
          touch(tch[0], c.rB, ((8 * (rb & 3) + row) * 1280 + (i - 10 * row) * 128) | (lane < 40 ? 0u : OOB), 40960u * cg);
        }
        if constexpr (G == 119) {
          asm volatile("; HIDOK %0" : "+v"(cpf));
          PINS(v);
          ft = v >> 1; ntw = cg & 7; ci_ = v / 5; ct = 5 * ntw + v - 5 * ci_;
          hasw = lt_mask<54>(ft) & ((v & 1) - 1u); hasc = lt_mask<100>(v);
        }
        if constexpr (G >= 121 && G < 131 && (G & 1)) {
          constexpr int i = (G - 121) >> 1;
          const unsigned o = 1024 * i + 16 * lane, row = o / 160;
          pv[i] = row * 1120 + o; PINV(pv[i]);
        }
        if constexpr (G == 133) {
          vow = vo | (OOB & ~hasw); voc = vo | (OOB & ~hasc); PINV(vow); PINV(voc);
          sow = BT_OFF + (unsigned)((ntw * KS2 + ft) * 5) * 1024u; PINS(sow);
        }
        if constexpr (G == 135) { soc = BT_OFF + (unsigned)((ntw * KS2 + 56 + 2 * ci_) * 5 + ct - 5 * ntw) * 1024u; PINS(soc); }
        if constexpr (G == 101) {
          PINS(c.rho);
          const unsigned base = (unsigned)(32 * (cg + 5) + 16 * (c.w & 1)) * 1280u;
          st2.k = ssel(mH, (unsigned)(16 + cg) * 128u + c.rho * (128u * 3456u), base + c.rho * 256u);
          st2.end = ssel(mH, 640u * 3456u, base + 1280u);
          PINS(st2.k); PINS(st2.end);
        }
        if constexpr (G == 103) {
          st2.adv = ssel(mH, 64u * 3456u, 256u); st2.lim = ssel(mH, 640u * 3456u, 1280u); st2.d1 = ssel(mH, 8u * 3456u - 1024u, 4096u);
          PINS(st2.adv); PINS(st2.lim); PINS(st2.d1);
        }
        if constexpr (G == 105) { st2.d2 = ssel(mH, 0u, 8192u); st2.d3 = ssel(mH, 8u * 3456u - 1024u, 12288u); PINS(st2.d2); PINS(st2.d3); }
        if constexpr (G == 111) {
          const void* pb = psel(mW, p.bre, p.bim);
          const unsigned nb = ssel(m2, P_ * D_ * 2, 0);
          st2.ra = mkrs(psel(mH, p.w1, pb), ssel(mH, D_ * F_ * 2, nb));
          st2.rb = mkrs(psel(mH, (const char*)p.w3 - 2048, pb), ssel(mH, D_ * F_ * 2 + 2048, nb));
          PINS(st2.ra); PINS(st2.rb);
        }
        if constexpr (G == 107) {
          const unsigned hm = mH;
          st2.v0 = (c.vD & hm) | (c.vS0 & ~hm); st2.v1 = (c.vD & hm) | (c.vS1 & ~hm); PINV(st2.v0); PINV(st2.v1);
        }
        if constexpr (G == 109) {
          const unsigned hm = mH;
          st2.v2 = (c.vD & hm) | (c.vS2 & ~hm); st2.v3 = (c.vD & hm) | (c.vS3 & ~hm); PINV(st2.v2); PINV(st2.v3);
        }
        if constexpr (G == 157) { setm0<0>(tb); const RSRC rw = mkrs(p.gw, ssel(lt_mask<15>(cg), 0u, 1280u)); dma<0>(vo, rw, 0); dma<1024>(vo, rw, 0); }
        if constexpr (G == 131) { pv[5] = (unsigned)(16 * ct + (lane >> 2)) * 1280u + 64u * ci_ + 16u * (lane & 3); PINV(pv[5]); }
      },
      [&](auto I) {
        constexpr int i = I;
        if constexpr (i == -2) {
          {
            const __attribute__((address_space(4))) Args* ka = (const __attribute__((address_space(4))) Args*)__builtin_amdgcn_kernarg_segment_ptr();
            PINS(ka);
            p.bre = ka->bre; p.bim = ka->bim; p.cre = ka->cre; p.cim = ka->cim; p.dsk = ka->dsk; p.w2 = ka->w2; p.out = ka->out;
          }
  {
    u64 pc; unsigned len;
    asm volatile("s_getpc_b64 %0\n1:\n\ts_mov_b32 %1, fused_code_end-1b\n\ts_max_u32 %1, %1, fused_side_end-1b" : "=s"(pc), "=s"(len)::"scc");
    asm volatile("buffer_load_dword %0, %1, %2, 0 offen ; HIDLD" : "=v"(cpf) : "v"((r * 20 + w * 5 + lane) * 128u | (lane < 5 ? 0u : OOB)), "s"(mkrs((const void*)pc, CPF ? len : 0)) : "memory");
  }
  { const unsigned ch = (32 * blk1 + l32) * 2; raw[0] = ldu16(p.lre, ch); raw[1] = ldu16(p.lim, ch); raw[2] = ldu16(p.ldt, ch); }
  asm volatile("global_load_dwordx2 %0, %1, %2 ; HIDLD" : "=v"(tlp) : "v"((lane < 15 ? lane : 0) * 8), "s"(__builtin_amdgcn_kernarg_segment_ptr()) : "memory");
        } else if constexpr (i == -3) {
          asm volatile("" : "+s"(p.bre), "+s"(p.bim), "+s"(p.cre), "+s"(p.cim), "+s"(p.dsk), "+s"(p.w2), "+s"(p.out));
          c.rB = mkrs(psel(mW, p.bre, p.bim), P_ * D_ * 2);
        } else if constexpr (i < 0) {
          int ln = lane; PINV(ln);
          const unsigned m32 = ln & 31, hh = ln >> 5;
#pragma unroll
          for (int s = 0; s < 8; ++s) { sa[s] = c.lds0 + m32 * 256 + (((2 * s + hh) ^ (m32 & 15)) << 4); PINV(sa[s]); }
          const unsigned blk = (ln >> 4) & 1, q = (ln & 15) >> 2, pp = ln & 3;
          const unsigned row = 8 * hh + q, ch = 2 * blk + (pp >> 1);
          const unsigned a = c.lds0 + row * 128 + ((ch ^ (((row >> 1) & 1) << 2)) * 16) + 8 * (pp & 1);
          rbd[0] = a; rbd[1] = a ^ 64; rbd[2] = a + 49152; rbd[3] = (a ^ 64) + 49152;
          asm volatile("" : "+v"(rbd[0]), "+v"(rbd[1]), "+v"(rbd[2]), "+v"(rbd[3]));
          asm volatile("s_waitcnt vmcnt(%3) ; HIDOK %0 %1 %2" : "+v"(raw0[0]), "+v"(raw0[1]),  "+v"(raw0[2]) : "n"(12 + 4 * PORD) : "memory");
          par[0] = ssm_par(raw0[0], raw0[1], raw0[2]); pin_par(par[0]);
          PINS(ep);
        } else {
          setm0<8192 + 1024 * i>(tb);
          if constexpr (i < 5) dma<0>(pv[i], mkrs(p.w2, ssel(hasw, F_ * D_ * 2, 0)), (unsigned)(32 * ft) * 1280u + 160u * ntw);
          else dma<0>(pv[5], mkrs(i == 5 ? p.cre : p.cim, ssel(hasc, D_ * P_ * 2, 0)), 0);
        }
      });
  }
  STAMP(1);
  constexpr unsigned GL = 12288, DL = 13312;
  if (__builtin_expect(cg == 15, 0)) {
    wait_vm<0>();
    f32x16 a;
    unsigned gk = c.rho * 256u;
    sfor<5>([&](auto N) {
      const unsigned ad = tb + gk + 16 * h;
      bf16x8 gf[8];
#pragma unroll
      for (int s = 0; s < 8; ++s) gf[s] = ldsr(ad + 32 * s);
      sfor<8>([&](auto S) { if constexpr (N + S == 0) mf_xb0<0>(a, gf[S]); else mf_xb<8 * N + S>(a, gf[S]); });
      gk = gk == 1024u ? 0u : gk + 256u;
    });
    asm volatile("s_nop 7\n\ts_nop 7" : "+v"(a));
    const float lg = rbf(a[0]);
    const float g = rbf(1.f / (1.f + __expf(-lg)));
    __builtin_amdgcn_raw_buffer_store_b32(__float_as_uint(g), rws, (unsigned)(128 * rb + 32 * w + 16 * ((l32 >> 2) & 1) + 4 * (l32 >> 3) + (l32 & 3)) * 4u | (h ? OOB : 0u), G_OFF, SC1);
  }

  f32x16 zr[2], zi[2];
  {
    float ta[3], tp[2], hv[2];
    unsigned pk[4], pad;
    s16x4 t0, t1;
    u32x4 ca, cb2;
    const unsigned sof = mtbase + (unsigned)(2 * cg) * 4096u;
    s_body(zr[0], zi[0], st, c, sa,
      [&](Stream& s) { s = st2; },
      [&](auto NN) { if constexpr (NN == 2) st64(tag, rws, vlead | (OOB & lt_mask<15>(cg)), FG_ + rb * 128); },
      [&](auto GG) {
        constexpr int G = GG;
        constexpr int P = G / 5, ph = G % 5, a = 2 * P, b = a + 1;
        auto ga = [&](int e) { return accp[e >> 4][e & 15]; };
        auto up = [&](int e) { return accp[2 + (e >> 4)][e & 15]; };
        if constexpr (ph == 0) {
          ta[0] = o_mulk(ga(a)); ta[1] = o_mulk(ga(b));
          if constexpr (P > 0) { hv[1] = o_mul(tp[1], ta[2]); }
        }
        if constexpr (ph == 1) {
          ta[0] = o_exp(ta[0]);
          if constexpr (P > 0) {
            pk[(P - 1) & 3] = o_cvt(hv[0], hv[1]);
            if constexpr (((P - 1) & 3) == 3) st128((u32x4){pk[0], pk[1], pk[2], pk[3]}, rws, vst + (((P - 1) >> 2) & 1) * 512, sof + ((P - 1) >> 3) * 4096);
          }
        }
        if constexpr (ph == 2) { ta[1] = o_exp(ta[1]); ta[0] = o_add1(ta[0]); }
        if constexpr (ph == 3) { ta[0] = o_rcp(ta[0]); ta[1] = o_add1(ta[1]); tp[0] = o_mul(ga(a), up(a)); }
        if constexpr (ph == 4) { ta[2] = o_rcp(ta[1]); tp[1] = o_mul(ga(b), up(b)); hv[0] = o_mul(tp[0], ta[0]); }
        if constexpr (G == 79) {
          asm volatile("s_nop 0");
          hv[1] = o_mul(tp[1], ta[2]); pk[3] = o_cvt(hv[0], hv[1]);
          st128((u32x4){pk[0], pk[1], pk[2], pk[3]}, rws, vst + 512, sof + 4096);
        }
        if constexpr ((TCH & 2) && (G == 2 || G == 16)) {
          const unsigned vt = ((160 * (rb & 3) + 40 * w + lane) * 3456u) | (lane < 40 ? 0u : OOB) | (OOB & ~mH);
          if constexpr (G == 2) touch(tch[1], c.rw1, vt, 128u * (16 + cg)); else touch(tch[2], c.rw3, vt, 128u * (16 + cg) + 2048);
        }
        if constexpr (G == 61) {
          if constexpr (TCH & 1) asm volatile("; HIDOK %0" : "+v"(tch[0]));
          if constexpr (TCH & 2) asm volatile("; HIDOK %0 %1" : "+v"(tch[1]), "+v"(tch[2]));
        }
        if constexpr (G == 6) { const unsigned q = lane >> 4; pad = tb + 8192 + (16 * (q >> 1) + 4 * (q & 1) + ((lane & 15) >> 2)) * 160 + 8 * (lane & 3); PINV(pad); }
        if constexpr (G >= 30 && G < 55 && G % 5 == 1) { t0 = trr(pad + 32 * ((G - 30) / 5)); t1 = trr(pad + 32 * ((G - 30) / 5) + 1280); }
        if constexpr (G >= 35 && G < 60 && G % 5 == 0) st128(__builtin_bit_cast(u32x4, cat(t0, t1)), rws, vow, sow + 1024 * ((G - 35) / 5));
        if constexpr (G == 56) {
          const unsigned ad = tb + 8192 + 5120 + (lane & 15) * 64 + (lane >> 4) * 16;
          ca = __builtin_bit_cast(u32x4, ldsr(ad)); cb2 = __builtin_bit_cast(u32x4, ldsr(ad + 1024));
        }
        if constexpr (G == 66 || G == 71) {
          constexpr int o = G == 66 ? 0 : 2;
          const unsigned a0 = ca[o], a1 = ca[o + 1], b0 = cb2[o] ^ 0x80008000u, b1 = cb2[o + 1] ^ 0x80008000u;
          st128((u32x4){__builtin_amdgcn_perm(b0, a0, 0x05040100u), __builtin_amdgcn_perm(b0, a0, 0x07060302u),
                        __builtin_amdgcn_perm(b1, a1, 0x05040100u), __builtin_amdgcn_perm(b1, a1, 0x07060302u)}, rws, voc, soc + (o >> 1) * 5120);
        }
      });
  }
  STAMP(2);
  auto flag_ok = [&](u32x2 f) { return __all(f[0] == tag[0] && f[1] == tag[1]); };
  auto spin = [&](unsigned off) __attribute__((always_inline)) {
#pragma unroll 1
    for (unsigned spins = 0; spins < (1u << 20); ++spins) {
      if (flag_ok(ld64_sync(rws, off))) break;
      __builtin_amdgcn_s_sleep(SLP);
    }
  };
  constexpr int NS = 24, PRE = PRE_T + PRE_E;
  auto ldn = [&](auto N) __attribute__((always_inline)) {
    constexpr int n = N, s = kOrd.s[n], k = kOrd.k[n], R = 36 * (s % 7) + 4 * k;
    const unsigned va = s == 13 ? vo | (OOB & ~mW) : vo;
    if constexpr (k < 4) ld_frag<R, k * 1024>(rws, va, bbase);
    else if constexpr (k == 4) { s_step<4096>(bbase); ld_frag<R, 0>(rws, va, bbase); s_step<16384>(bbase); }
    else { ld_frag<R, (k - 5) * 1024>(rws, va, abase); if constexpr (k == 8) s_step<16384>(abase); }
  };
  auto ldr = [&](auto LO, auto HI) __attribute__((always_inline)) { sfor<HI - LO>([&](auto K) { ldn(IC<LO + K>{}); }); };
  constexpr int NZ1 = kOrd.firstA[8], NZ2 = kOrd.firstA[14];
  auto ld_g = [&]() { return ld128_async(rws, vo | (lane < 32 ? 0u : OOB), G_OFF + 512u * rb); };
  auto ld_st = [&](int blk, int u) { return ld128_async(rws, (unsigned)max(stb - 10240 * u, l32 * 16), __builtin_amdgcn_readfirstlane(ST_OFF + 512 * blk)); };
  auto put_st = [&](int blk, float a, float b) { st128((u32x4){h ? 0u : __float_as_uint(a), tag[0], h ? 0u : __float_as_uint(b), tag[1]}, rws, stv, __builtin_amdgcn_readfirstlane(ST_OFF + 512 * blk)); };
  auto st_late = [&](const u32x4& v, unsigned t) { return t | (v[1] ^ tag[0]) | (v[3] ^ tag[1]); };
  auto st_wait = [&](int blk, u32x4 (&sv)[8]) __attribute__((always_inline)) {
#pragma unroll 1
    for (unsigned spins = 0; spins < (1u << 20); ++spins) {
      unsigned late = 0;
#pragma unroll
      for (int u = 0; u < 8; ++u) sv[u] = ld_st(blk, u);
      wait_vm<0>(); hid_ok(sv);
#pragma unroll
      for (int u = 0; u < 8; ++u) late = st_late(sv[u], late);
      if (!__any(late != 0)) break;
      __builtin_amdgcn_s_sleep(SLP);
    }
  };
  auto tw_ad = [&](int j) { return (tw0 ^ ((((j & 3) >> 1) + 4 * ((j >> 2) & 1)) << 4)) + ((j & 3) + 8 * (j >> 2)) * 128; };
  auto tr_ad = [&](int q) { return (trd ^ ((q & 1) << 4)) + (q >> 1) * 2048; };
  const int seg = 2 * w + h;

  float sr, si, u0, u1, lr_, li_, pr, pi, nr, ni, qr, qi;
  f32x2 sg[8];
  auto scan_a = [&](auto GG, auto NRD) {
    constexpr int G = GG, NR = NRD;
        if constexpr (G == 1) { asm volatile("" : "+v"(zr[0]), "+v"(zi[0])); sr = zr[0][0]; si = zi[0][0]; }
        if constexpr (G >= 1 && G < 31) {
          constexpr int j = ((G - 1) >> 1) + 1;
          if constexpr (G & 1) { u0 = o_fnma(par[0].li, si, zr[0][j]); u1 = o_fma(par[0].li, sr, zi[0][j]); }
          else { sr = o_fma(par[0].lr, sr, u0); si = o_fma(par[0].lr, si, u1); }
        }
        if constexpr (G == 30) *(LAS f32x2*)(size_t)segw = (f32x2){sr, si};
        if constexpr (G == 35) asm volatile("s_waitcnt lgkmcnt(%0)\n\ts_barrier" ::"n"(5 * NR) : "memory");
        if constexpr (G >= 35 && G < 39) { sg[2 * (G - 35)] = *(const LAS f32x2*)(size_t)(segr + 512 * (G - 35)); sg[2 * (G - 35) + 1] = *(const LAS f32x2*)(size_t)(segr + 512 * (G - 35) + 256); }
        if constexpr (G == 38) { sr = sg[0][0]; si = sg[0][1]; lr_ = 0.f; li_ = 0.f; }
        if constexpr (G >= 38 && G < 59) {
          constexpr int j = (G - 38) / 3 + 1, ph = (G - 38) % 3;
          if constexpr (ph == 0) { u0 = o_fnma(par[0].mi, si, sg[j][0]); u1 = o_fma(par[0].mi, sr, sg[j][1]); }
          if constexpr (ph == 1) { const u64 m = __builtin_amdgcn_ballot_w64(seg == j); lr_ = o_sel(lr_, sr, m); li_ = o_sel(li_, si, m); }
          if constexpr (ph == 2) { sr = o_fma(par[0].mr, sr, u0); si = o_fma(par[0].mr, si, u1); }
        }
        if constexpr (G == 58) put_st(cg, sr, si);
        if constexpr (G >= 59 && G < 80) {
          constexpr int k = G < 74 ? (G - 59) / 5 : 3 + (G - 74) / 2, ph = G < 74 ? (G - 59) % 5 : 3 + (G - 74) % 2;
          if constexpr (G == 59) { nr = par[0].mr; ni = par[0].mi; pr = 1.f; pi = 0.f; }
          if constexpr (G == 74) { qr = nr; qi = ni; }
          float& a = k < 3 ? nr : qr; float& b = k < 3 ? ni : qi;
          if constexpr (ph == 0) { u0 = o_mul(pi, b); u1 = o_mul(pi, a); }
          if constexpr (ph == 1) { u0 = o_fms(pr, a, u0); u1 = o_fma(pr, b, u1); }
          if constexpr (ph == 2) { const u64 m = __builtin_amdgcn_ballot_w64((seg >> k) & 1); pr = o_sel(pr, u0, m); pi = o_sel(pi, u1, m); }
          if constexpr (ph == 3) { u0 = o_mul(b, b); u1 = o_mul(a, b); }
          if constexpr (ph == 4) { a = o_fms(a, a, u0); b = o_add(u1, u1); }
        }
  };
  f32x4 gt[4];
  auto scan_fin = [&](u32x4 (&sv)[8], int blk, const Par& pa, const f32x16& xr, const f32x16& xi, auto extra) __attribute__((always_inline)) {
    unsigned late = 0;
#pragma unroll
    for (int u = 0; u < 8; ++u) late = st_late(sv[u], late);
    if (__any(late != 0)) st_wait(blk, sv);
    sr = __uint_as_float(sv[7][0]); si = __uint_as_float(sv[7][2]);
#pragma unroll
    for (int u = 6; u >= 0; --u) {
      u0 = o_fnma(ni, si, __uint_as_float(sv[u][0])); u1 = o_fma(ni, sr, __uint_as_float(sv[u][2]));
      sr = o_fma(nr, sr, u0); si = o_fma(nr, si, u1);
    }
    asm volatile("s_nop 1" : "+v"(sr), "+v"(si));
    auto a = __builtin_amdgcn_permlane32_swap(__float_as_uint(sr), __float_as_uint(sr), false, false);
    auto b = __builtin_amdgcn_permlane32_swap(__float_as_uint(si), __float_as_uint(si), false, false);
    sr = __uint_as_float(a[1]); si = __uint_as_float(b[1]);
    u0 = o_fnma(qi, si, __uint_as_float(a[0])); u1 = o_fma(qi, sr, __uint_as_float(b[0]));
    sr = o_fma(qr, sr, u0); si = o_fma(qr, si, u1);
    u0 = o_fnma(pi, si, lr_); u1 = o_fma(pi, sr, li_);
    sr = o_fma(pr, sr, u0); si = o_fma(pr, si, u1);
    float e0, e1, y0, y1;
    sfor<17>([&](auto JJ) {
      constexpr int j = JJ;
      if constexpr (j < 16) { u0 = o_fnma(pa.li, si, xr[j]); u1 = o_fma(pa.li, sr, xi[j]); }
      if constexpr (j > 0) { y0 = o_fms(e0, sr, y0); y1 = o_fma(e0, si, y1); }
      if constexpr (j < 16) { sr = o_fma(pa.lr, sr, u0); si = o_fma(pa.lr, si, u1); e0 = o_mul(pa.cr, gt[j >> 2][j & 3]); e1 = o_mul(pa.ci, gt[j >> 2][j & 3]); }
      if constexpr (j > 0) *(LAS unsigned*)(size_t)tw_ad(j - 1) = o_cvt(y0, y1);
      if constexpr (j < 16) { y0 = o_mul(e1, si); y1 = o_mul(e1, sr); extra(JJ); }
      SB();
    });
    bf16x8 tf[4];
#pragma unroll
    for (int q = 0; q < 4; ++q) tf[q] = ldsr(tr_ad(q));
#pragma unroll
    for (int q = 0; q < 4; ++q) st128(__builtin_bit_cast(u32x4, tf[q]), rws, vo, mtbase + (unsigned)(56 + 2 * blk + (q & 1)) * 4096u + (q >> 1) * 1024);
  };

  if (__builtin_expect(heavy, 1)) {
    f32x16 acc[4];
    u32x2 fav;
    u32x4 gq, sv[8];
    f32x2 zz[16];
    unsigned late;
    d_body<false, (15 * PRE_T + 50) / 51, (31 * PRE_T + 50) / 51>(acc, st, c, rbd, sa, 0u, [&](Stream&) {},
      [&](auto NN) {
        constexpr int n = NN;
        if constexpr (n == 2) st64(tag, rws, vlead, FA_ + myfl);
        if constexpr (n == 7) {
          asm volatile("s_waitcnt vmcnt(12) ; HIDOK %0" : "+v"(fav)::"memory");
          if (__builtin_expect(!flag_ok(fav), 0)) spin(fa_off);
          asm volatile("" ::: "memory");
        }
        if constexpr (n == 8) { hid_ok(sv); hid_ok(gq); }
      },
      [&](auto GG) {
        constexpr int G = GG;
        scan_a(GG, IC<2>{});
        if constexpr (G >= 80 && G < 96) { zz[G - 80] = (f32x2){zr[0][G - 80], zi[0][G - 80]}; PINV(zz[G - 80]); }
        if constexpr (G == 89) fav = ld64_async(rws, fa_off);
        if constexpr (G == 108) gq = ld_g();
        if constexpr (G >= 100 && G < 108) sv[107 - G] = ld_st(cg, 107 - G);
        if constexpr (G >= 109) { constexpr int i = ((G - 109) * PRE_T + 50) / 51; if constexpr (i < PRE_T && (51 * i) / PRE_T == G - 109) ldn(IC<i < PRE_T ? i : 0>{}); }
        if constexpr (G == 125) *(LAS u32x4*)(size_t)(tb + GL + vo) = gq;
        if constexpr (G >= 126 && G < 134) { if constexpr (G == 126) late = 0; late = st_late(sv[133 - G], late); PINV(late); }
        if constexpr (G == 134) {
          if (__builtin_expect(__any(late != 0), 0)) st_wait(cg, sv);
          sr = __uint_as_float(sv[7][0]); si = __uint_as_float(sv[7][2]);
        }
        if constexpr (G >= 134 && G < 148) {
          constexpr int u = 6 - ((G - 134) >> 1);
          if constexpr ((G & 1) == 0) { u0 = o_fnma(ni, si, __uint_as_float(sv[u][0])); u1 = o_fma(ni, sr, __uint_as_float(sv[u][2])); }
          else { sr = o_fma(nr, sr, u0); si = o_fma(nr, si, u1); }
        }
        if constexpr (G == 149) {
          auto a = __builtin_amdgcn_permlane32_swap(__float_as_uint(sr), __float_as_uint(sr), false, false);
          auto b = __builtin_amdgcn_permlane32_swap(__float_as_uint(si), __float_as_uint(si), false, false);
          sr = __uint_as_float(a[1]); si = __uint_as_float(b[1]); u0 = __uint_as_float(a[0]); u1 = __uint_as_float(b[0]);
          PINV(sr); PINV(si); PINV(u0); PINV(u1);
        }
        if constexpr (G == 150) { u0 = o_fnma(qi, si, u0); u1 = o_fma(qi, sr, u1); }
        if constexpr (G == 151) { sr = o_fma(qr, sr, u0); si = o_fma(qr, si, u1); }
        if constexpr (G == 152) { u0 = o_fnma(pi, si, lr_); u1 = o_fma(pi, sr, li_); }
        if constexpr (G == 153) { sr = o_fma(pr, sr, u0); si = o_fma(pr, si, u1); }
        if constexpr (G >= 154 && G < 158) gt[G - 154] = *(const LAS f32x4*)(size_t)(tb + GL + (32 * w + 16 * h + 4 * (G - 154)) * 4);
      },
      [&](auto) {});
    STAMP(3);
    const unsigned sof = mtbase + (unsigned)(32 + 2 * cg) * 4096u;
    unsigned pk[16];
    f32x2 sc = {sr, si};
    const f32x2 lam = {par[0].lr, par[0].li}, cf = {par[0].cr, par[0].ci};
    sfor<16>([&](auto JJ) {
      constexpr int j = JJ, t = j >> 3, a = (2 * j) & 15, b = a + 1;
      unsigned hz;
      seam_slot<j & 1>(sc, zz[j], lam, cf, (f32x2){gt[j >> 2][j & 2], gt[j >> 2][(j & 2) + 1]}, (f32x2){acc[t][a], acc[t][b]}, (f32x2){acc[2 + t][a], acc[2 + t][b]}, hz, pk[j]);
      *(LAS unsigned*)(size_t)tw_ad(j) = hz;
      ldr(IC<PRE_T + j * PRE_E / 16>{}, IC<PRE_T + (j + 1) * PRE_E / 16>{});
      SB();
    });
    bf16x8 tf[4];
#pragma unroll
    for (int q = 0; q < 4; ++q) tf[q] = ldsr(tr_ad(q));
#pragma unroll
    for (int q = 0; q < 4; ++q) st128((u32x4){pk[4 * q], pk[4 * q + 1], pk[4 * q + 2], pk[4 * q + 3]}, rws, vst + (q & 1) * 512, sof + (q >> 1) * 4096);
#pragma unroll
    for (int q = 0; q < 4; ++q) st128(__builtin_bit_cast(u32x4, tf[q]), rws, vo, mtbase + (unsigned)(56 + 2 * cg + (q & 1)) * 4096u + (q >> 1) * 1024);
  } else {
    u32x2 fav;
    u32x4 sv[8];
    if (two) {
      s_body(zr[1], zi[1], st, c, sa, [&](Stream& s) { stream_null(s, c); },
             [&](auto NN) { if constexpr (NN == 2) st64(tag, rws, vlead, FA_ + myfl); },
             [&](auto GG) {
               scan_a(GG, IC<1>{});
               if constexpr (GG == 78) { par[1] = ssm_par(raw[0], raw[1], raw[2]); pin_par(par[1]); }
               if constexpr (GG == 75) fav = ld64_async(rws, fa_off);
             });
    } else {
      wait_bar<0>();
      st64(tag, rws, vlead, FA_ + myfl);
      fav = ld64_async(rws, fa_off);
      zr[1] = zr[0]; zi[1] = zi[0]; par[1] = par[0];
      lr_ = li_ = pr = pi = nr = ni = qr = qi = 0.f;
    }
    asm volatile("s_nop 7\n\ts_nop 7" : "+v"(zr[1]), "+v"(zi[1]));
    STAMP(3);
    BST(32);
    float b0 = zr[1][0], b1 = zi[1][0];
#pragma unroll
    for (int j = 1; j < 16; ++j) {
      u0 = o_fnma(par[1].li, b1, zr[1][j]); u1 = o_fma(par[1].li, b0, zi[1][j]);
      b0 = o_fma(par[1].lr, b0, u0); b1 = o_fma(par[1].lr, b1, u1);
    }
    *(LAS f32x2*)(size_t)(segw + 2048) = (f32x2){b0, b1};
    asm volatile("s_waitcnt vmcnt(0) lgkmcnt(0) ; HIDOK %0\n\ts_barrier" : "+v"(fav)::"memory");
#pragma unroll
    for (int g = 0; g < 8; ++g) sg[g] = *(const LAS f32x2*)(size_t)(segr + 2048 + g * 256);
#pragma unroll
    for (int u = 7; u >= 0; --u) sv[u] = ld_st(cg, u);
    SB();
    float l0 = 0.f, l1 = 0.f;
    b0 = sg[0][0]; b1 = sg[0][1];
#pragma unroll
    for (int g = 1; g < 8; ++g) {
      u0 = o_fnma(par[1].mi, b1, sg[g][0]); u1 = o_fma(par[1].mi, b0, sg[g][1]);
      const u64 m = __builtin_amdgcn_ballot_w64(seg == g);
      l0 = o_sel(l0, b0, m); l1 = o_sel(l1, b1, m);
      b0 = o_fma(par[1].mr, b0, u0); b1 = o_fma(par[1].mr, b1, u1);
    }
    put_st(blk1, b0, b1);
    if (__builtin_expect(!flag_ok(fav), 0)) spin(fa_off);
    asm volatile("" ::: "memory");
    u32x4 gq = ld_g();
    ldr(IC<0>{}, IC<9>{});
    SB();
    float n0 = par[1].mr, n1 = par[1].mi, p0 = 1.f, p1 = 0.f;
#pragma unroll
    for (int k = 0; k < 3; ++k) {
      const float a = p0 * n0 - p1 * n1, b = p0 * n1 + p1 * n0;
      if ((seg >> k) & 1) { p0 = a; p1 = b; }
      csq(n0, n1);
    }
    float q0 = n0, q1 = n1;
    csq(q0, q1); csq(q0, q1); csq(q0, q1);
    asm volatile("" : "+v"(n0), "+v"(n1), "+v"(q0), "+v"(q1), "+v"(p0), "+v"(p1));
    wait_vm<9>(); hid_ok(sv); hid_ok(gq);
    *(LAS u32x4*)(size_t)(tb + GL + vo) = gq;
#pragma unroll
    for (int u = 0; u < 4; ++u) gt[u] = *(const LAS f32x4*)(size_t)(tb + GL + (32 * w + 16 * h + 4 * u) * 4);
    BST(33);
    if (two) scan_fin(sv, cg, par[0], zr[0], zi[0], [&](auto JJ) { ldr(IC<9 + JJ * 3 / 2>{}, IC<9 + (JJ + 1) * 3 / 2>{}); });
    else {
      ldr(IC<9>{}, IC<33>{});
    }
    BST(35);
#pragma unroll
    for (int u = 7; u >= 0; --u) sv[u] = ld_st(blk1, u);
    ldr(IC<33>{}, IC<36>{});
    wait_vm<3>(); hid_ok(sv);
    lr_ = l0; li_ = l1; nr = n0; ni = n1; pr = p0; pi = p1; qr = q0; qi = q1;
    SB();
    scan_fin(sv, blk1, par[1], zr[1], zi[1], [&](auto JJ) {
      ldr(IC<36 + JJ * (PRE - 36) / 16>{}, IC<36 + (JJ + 1) * (PRE - 36) / 16>{});
    });
    asm volatile("fused_side_end:");
  }
  STAMP(5);
  BST(60);

  {
    f32x4 acc[4][5];
    u32x2 fz;
    float gv[4];
    unsigned orow[5], oad[5], xrow, xt, fz_off;
    u32x2 xv, dv;
    float gm;
    f32x4 sk[5];
    auto tau = [](int i) { return (i & 32) + 16 * ((i >> 2) & 1) + 4 * ((i >> 3) & 3) + (i & 3); };
    unsigned redw;
    SB();
    sfor<NS * 10>([&](auto QQ) {
      constexpr int Q = QQ, s = Q / 10, g = Q % 10;
      constexpr int n1 = ld_sched(Q, PRE), n0_ = Q ? ld_sched(Q - 1, PRE) : PRE - 1, n0 = n0_ > 215 ? 215 : n0_;
      constexpr bool go = n1 > n0 && n1 < 216;
      if constexpr (g == 0) BST(64 + s);
      if constexpr (g == 0 && s != 23) {
        constexpr int cnt = n0 - kOrd.last[s == 22 ? 23 : s];
        static_assert(cnt >= 0, "a step's loads must be out before it starts");
        wait_vm<(cnt > 60 ? 60 : cnt)>();
      }
      static_assert(!go || kOrd.s[go ? n1 : 0] < s + 7, "register slot still in use");
      if constexpr (Q == QZ) {
        wait_bar<n0 - PRE + 1>();
        st64(tag, rws, vlead, FZ_ + myfl);
        SB();
      }
      if constexpr (Q == 100) {
        if (__builtin_expect(bid == 249 && w == 0, 0)) {
#pragma unroll 1
          for (unsigned spins = 0; spins < (1u << 20); ++spins) {
            u32x2 f[4];
#pragma unroll
            for (int u = 0; u < 4; ++u) f[u] = ld64_async(rws, FA_ + (64 * u + lane) * 128);
            asm volatile("s_waitcnt vmcnt(0) ; HIDOK %0 %1 %2 %3" : "+v"(f[0]), "+v"(f[1]), "+v"(f[2]), "+v"(f[3])::"memory");
            if (flag_ok(f[0]) && flag_ok(f[1]) && flag_ok(f[2]) && flag_ok(f[3])) break;
          }
          const u64 e1 = ep + 1;
          st64((u32x2){(unsigned)e1, (unsigned)(e1 >> 32)}, rws, vlead, EP_);
        }
      }
      if constexpr (go && (n1 == NZ1 || n1 == NZ2)) {
        BST(n1 == NZ1 ? 94 : 96);
        asm volatile("s_waitcnt vmcnt(10) ; HIDOK %0" : "+v"(fz)::"memory");
        if (__builtin_expect(!flag_ok(fz), 0)) spin(fz_off);
        asm volatile("" ::: "memory");
        BST(n1 == NZ1 ? 95 : 97);
        SB();
      }
      auto mm = [&](auto U) {
        constexpr int q = Q >= 220 ? Q - 220 : 2 * g + U, m = q / 5, n = q % 5, R = 36 * ((Q >= 220 ? 22 + U : s) % 7);
        if constexpr (s == 0) mf16_0<R + 4 * n, R + 20 + 4 * m>(acc[m][n]); else mf16<R + 4 * n, R + 20 + 4 * m>(acc[m][n]);
      };
      if constexpr (Q >= 225) *(LAS f32x4*)(size_t)(redw + (Q - 225) * 1024) = acc[(Q - 225) / 5][(Q - 225) % 5];
      mm(IC<0>{});
      SB();
      if constexpr (Q == 3) { fz_off = FZ_ + (rb * 16 + min(lane, 10)) * 128; PINV(fz_off); }
      if constexpr (go && n1 == NZ2 - 16) { fz_off = FZ_ + (rb * 16 + (lane & 15)) * 128; PINV(fz_off); }
      if constexpr (go && (n1 == NZ1 - 10 || n1 == NZ2 - 10)) fz = ld64_async(rws, fz_off);
      if constexpr (Q == 48) { xt = tb + GL + (64 * (idx & 1) + tau(lane & 15)) * 4; PINV(xt); }
      if constexpr (Q >= 50 && Q < 54) gv[Q - 50] = *(const LAS float*)(size_t)(xt + (((Q - 50) & 1) * 8 + ((Q - 50) >> 1) * 32) * 4);
      if constexpr (Q >= 56 && Q < 64 && Q % 2 == 0) { gv[(Q - 56) / 2] = rbf(1.f - gv[(Q - 56) / 2]); PINV(gv[(Q - 56) / 2]); }
      if constexpr (Q >= 135 && Q < 145) {
        sfor<2>([&](auto U) {
          constexpr int q = 2 * (Q - 135) + U, m = q / 5, n = q % 5;
#pragma unroll
          for (int e = 0; e < 4; ++e) acc[m][n][e] = o_mul(acc[m][n][e], gv[m]);
        });
      }
      if constexpr (Q >= 72 && Q < 96) {
        constexpr int n = (Q - 72) / 8, ph = (Q - 72) % 8;
        if constexpr (ph == 0) { xrow = (64 * n + lane) / 10; PINV(xrow); }
        if constexpr (ph == 2) { xt = tau(16 * w + (xrow & 15)); PINV(xt); }
        if constexpr (ph == 4) { xrow = (xt * 1280u + (1024 * n + 16 * lane - xrow * 160)) | (xrow < 16 ? 0u : OOB); PINV(xrow); }
        if constexpr (ph == 6) { setm0<8192 + 1024 * n>(tb); dma<0>(xrow, c.rx, (unsigned)(64 * mt) * 1280u + 160u * nt); }
      }
      if constexpr (Q == 98) { setm0<DL>(tb); dma<0>(vo | (lane < 10 ? 0u : OOB), mkrs(p.dsk, D_ * 2), 160u * nt); }
      if constexpr (Q >= 110 && Q < 130 && Q % 4 == 2) {
        constexpr int n = (Q - 110) / 4;
        const unsigned i = 64 * n + lane, row = i / 20, ch = i - 20 * row;
        orow[n] = row | (ch << 8); PINV(orow[n]);
      }
      if constexpr (Q == 132) { redw = c.lds0 + w * 20480 + (((lane + 4 * (lane >> 4)) & 15) + (lane & 48)) * 16; PINV(redw); }
      if constexpr (Q >= 150 && Q < 210) {
        constexpr int n = (Q - 150) / 12, ph = (Q - 150) % 12;
        if constexpr (ph == 0) {
          xv = *(const LAS u32x2*)(size_t)(tb + 8192 + 512 * n + 8 * lane); dv = *(const LAS u32x2*)(size_t)(tb + DL + 8 * (orow[n] >> 8));
          oad[n] = tau(16 * w + (orow[n] & 255)); PINV(oad[n]);
        }
        if constexpr (ph == 2) {
          gm = *(const LAS float*)(size_t)(tb + GL + (64 * (idx & 1) + oad[n]) * 4);
          oad[n] = (oad[n] * 640 + 4 * (orow[n] >> 8)) * 4u; PINV(oad[n]);
        }
        if constexpr (ph == 3) {
          const unsigned row = orow[n] & 255, ch = orow[n] >> 8;
          orow[n] = c.lds0 + ((w * 5 + (ch >> 2)) * 64 + ((row + 4 * (ch & 3)) & 15) + 16 * (ch & 3)) * 16; PINV(orow[n]);
        }
        if constexpr (ph >= 5 && (ph & 1)) {
          constexpr int e = (ph - 5) / 2;
          const float a = (e & 1) ? __uint_as_float(xv[e >> 1] & 0xffff0000u) : bf2f(xv[e >> 1]), b = (e & 1) ? __uint_as_float(dv[e >> 1] & 0xffff0000u) : bf2f(dv[e >> 1]);
          float t = o_mul(gm, rbf(o_mul(a, b)));
          sk[n][e] = t;
        }
      }
      SB();
      mm(IC<1>{});
      SB();
      if constexpr (go) ldn(IC<go ? n1 : 0>{});
      SB();
    });
    STAMP(7);
    BST(88);
    asm volatile("s_nop 7\n\ts_nop 7" : "+v"(acc[3][0]), "+v"(acc[3][1]), "+v"(acc[3][2]), "+v"(acc[3][3]), "+v"(acc[3][4]));
#pragma unroll
    for (int n = 0; n < 5; ++n) *(LAS f32x4*)(size_t)(redw + (15 + n) * 1024) = acc[3][n];
    asm volatile("s_waitcnt lgkmcnt(0)\n\ts_barrier" ::: "memory");
    const RSRC ro = mkrs(p.out, L_ * D_ * 4);
    f32x4 pt[5][4];
#pragma unroll
    for (int n = 0; n < 5; ++n)
#pragma unroll
      for (int u = 0; u < 4; ++u) pt[n][u] = *(const LAS f32x4*)(size_t)(orow[n] + u * 20480);
    SB();
#pragma unroll
    for (int n = 0; n < 5; ++n)
      st128(__builtin_bit_cast(u32x4, ((pt[n][0] + pt[n][1]) + (pt[n][2] + pt[n][3])) + sk[n]), ro, oad[n], (unsigned)(64 * mt * 640 + 80 * nt) * 4u);
  }
  STAMP(8);
  asm volatile("fused_code_end:");
}

extern "C" void kernel_launch(void* const* d_in, const int* in_sizes, int n_in, void* d_out, int out_size, void* d_ws, size_t ws_size, hipStream_t stream) {
  Args a;
  a.x = (const u16*)d_in[0]; a.lre = (const u16*)d_in[1]; a.lim = (const u16*)d_in[2]; a.bre = (const u16*)d_in[3]; a.bim = (const u16*)d_in[4];
  a.cre = (const u16*)d_in[5]; a.cim = (const u16*)d_in[6]; a.ldt = (const u16*)d_in[7]; a.dsk = (const u16*)d_in[8];
  a.w1 = (const u16*)d_in[10]; a.w3 = (const u16*)d_in[11]; a.w2 = (const u16*)d_in[12]; a.gw = (const u16*)d_in[13];
  a.ws = (char*)d_ws; a.out = (float*)d_out;
  (void)hipFuncSetAttribute((const void*)fused, hipFuncAttributeMaxDynamicSharedMemorySize, LDS1);
  fused<<<256, 256, LDS1, stream>>>(a);
}
```
